```python
import torch

def _softmax(x, axis=-1):
    return torch.softmax(x, dim=axis)
def _gelu(x):
    return torch.nn.functional.gelu(x, approximate="tanh")
def _silu(x):
    return torch.nn.functional.silu(x)
def _sigmoid(x):
    return torch.sigmoid(x)
def _layernorm(x, axis=-1, eps=1e-5):
    m = x.mean(dim=axis, keepdim=True)
    v = x.var(dim=axis, keepdim=True, unbiased=False)
    return (x - m) / torch.sqrt(v + eps)
def _scatter_add(tgt, idx, src, axis=0):
    return tgt.clone().index_add_(axis, idx, src)
def _segment_sum(src, seg, n):
    out = torch.zeros((n,) + src.shape[1:], dtype=src.dtype)
    return out.index_add_(0, seg, src)
def _pad(x, pw):
    flat = tuple(v for pair in reversed(pw) for v in pair)
    return torch.nn.functional.pad(x, flat)

kernel_name = 'standalone__mixer_channel_mlp__prompt'

def setup_inputs():

    _w = (lambda a: (lambda t: t.to(torch.bfloat16) if t.is_floating_point() else t)(torch.as_tensor(a)))
    B, S, D, F = 1, 2048, 576, 4608
    return {
        "x": _w((lambda *s: torch.randn(*s, dtype=torch.bfloat16) * 0.05)(B, S, D)),
        "w1": _w((lambda *s: torch.randn(*s, dtype=torch.bfloat16) * 0.8)(D, F) * (D ** -0.5)),
        "b1": _w((lambda *s: torch.randn(*s, dtype=torch.bfloat16) * 0.4)(F)),
        "w2": _w((lambda *s: torch.randn(*s, dtype=torch.bfloat16) * 0.05)(F, D) * (F ** -0.5)),
        "b2": _w((lambda *s: torch.randn(*s, dtype=torch.bfloat16) * 0.05)(D)),
    }

def reference(x, w1, b1, w2, b2):
    y = _layernorm(x, axis=-1)
    h = _gelu(torch.einsum("bsd,df->bsf", y, w1) + b1)
    return torch.einsum("bsf,fd->bsd", h, w2) + b2
```

```cpp
#include <hip/hip_runtime.h>
#include <hip/hip_bf16.h>

typedef unsigned short u16;
using bf16x8 = __attribute__((ext_vector_type(8))) short;
using f32x4 = __attribute__((ext_vector_type(4))) float;

constexpr int S_ = 2048, D_ = 576, F_ = 4608;

__device__ __forceinline__ float bf2f(u16 v) { return __uint_as_float(((unsigned)v) << 16); }
__device__ __forceinline__ u16 f2bf(float f) { __hip_bfloat16 b = __float2bfloat16(f); return *reinterpret_cast<u16*>(&b); }

__global__ void ln_kernel(const u16* __restrict__ x, u16* __restrict__ y) {
    int row = blockIdx.x * 4 + (threadIdx.x >> 6);
    int lane = threadIdx.x & 63;
    const u16* xr = x + (size_t)row * D_;
    float v[9]; float s = 0.f;
    for (int i = 0; i < 9; ++i) { v[i] = bf2f(xr[lane + 64 * i]); s += v[i]; }
    for (int o = 32; o; o >>= 1) s += __shfl_xor(s, o);
    float m = s * (1.f / D_);
    float q = 0.f;
    for (int i = 0; i < 9; ++i) { float d = v[i] - m; q += d * d; }
    for (int o = 32; o; o >>= 1) q += __shfl_xor(q, o);
    float r = rsqrtf(q * (1.f / D_) + 1e-5f);
    for (int i = 0; i < 9; ++i) y[(size_t)row * D_ + lane + 64 * i] = f2bf((v[i] - m) * r);
}

template <bool GELU>
__global__ __launch_bounds__(256) void gemm_kernel(const u16* __restrict__ A, const u16* __restrict__ B, const u16* __restrict__ bias,
                                                   u16* __restrict__ C, int M, int N, int K) {
    __shared__ u16 As[64][40];
    __shared__ u16 Bs[64][40];
    int tid = threadIdx.x, lane = tid & 63, w = tid >> 6;
    int bm = blockIdx.y * 64, bn = blockIdx.x * 64;
    int wm = (w >> 1) * 32, wn = (w & 1) * 32;
    f32x4 acc[2][2] = {};
    for (int k0 = 0; k0 < K; k0 += 32) {
        {
            int r = tid >> 2, c = (tid & 3) * 8;
            *(bf16x8*)&As[r][c] = *(const bf16x8*)&A[(size_t)(bm + r) * K + k0 + c];
            int kr = tid >> 3, nc = (tid & 7) * 8;
            bf16x8 v = *(const bf16x8*)&B[(size_t)(k0 + kr) * N + bn + nc];
            for (int j = 0; j < 8; ++j) Bs[nc + j][kr] = v[j];
        }
        __syncthreads();
        bf16x8 a[2], b[2];
        for (int i = 0; i < 2; ++i) a[i] = *(bf16x8*)&As[wm + i * 16 + (lane & 15)][(lane >> 4) * 8];
        for (int i = 0; i < 2; ++i) b[i] = *(bf16x8*)&Bs[wn + i * 16 + (lane & 15)][(lane >> 4) * 8];
        for (int i = 0; i < 2; ++i) for (int j = 0; j < 2; ++j)
            acc[i][j] = __builtin_amdgcn_mfma_f32_16x16x32_bf16(a[i], b[j], acc[i][j], 0, 0, 0);
        __syncthreads();
    }
    for (int i = 0; i < 2; ++i) for (int j = 0; j < 2; ++j) for (int r = 0; r < 4; ++r) {
        int row = bm + wm + i * 16 + (lane >> 4) * 4 + r, col = bn + wn + j * 16 + (lane & 15);
        float v = acc[i][j][r] + bf2f(bias[col]);
        if (GELU) {
            float z = v * (-2.302208198f + -0.1029432397f * v * v);
            v = v / (1.f + exp2f(z));
        }
        C[(size_t)row * N + col] = f2bf(v);
    }
}

extern "C" void kernel_launch(void* const* d_in, const int* in_sizes, int n_in,
                              void* d_out, int out_size, void* d_ws, size_t ws_size,
                              hipStream_t stream) {
    const u16* x = (const u16*)d_in[0];
    const u16* w1 = (const u16*)d_in[1];
    const u16* b1 = (const u16*)d_in[2];
    const u16* w2 = (const u16*)d_in[3];
    const u16* b2 = (const u16*)d_in[4];
    u16* y = (u16*)d_ws;
    u16* h = y + (size_t)S_ * D_;
    ln_kernel<<<S_ / 4, 256, 0, stream>>>(x, y);
    gemm_kernel<true><<<dim3(F_ / 64, S_ / 64), 256, 0, stream>>>(y, w1, b1, h, S_, F_, D_);
    gemm_kernel<false><<<dim3(D_ / 64, S_ / 64), 256, 0, stream>>>(h, w2, b2, (u16*)d_out, S_, D_, F_);
}
```

```cpp
#include <hip/hip_runtime.h>
#include <hip/hip_bf16.h>

__global__ __launch_bounds__(512, 2) void mixer_mlp_kernel(const void* x, const void* w1, const void* b1, const void* w2,
                                                           const void* b2, void* out, void* ws) {
    __shared__ __attribute__((aligned(16))) char smem[163840];
    const unsigned bid = blockIdx.x;
    const unsigned wv = __builtin_amdgcn_readfirstlane(threadIdx.x >> 6);
    asm volatile(
        "LSTART_%=:\n"
        "s_load_dwordx8 s[4:11], %0, 0x0\n"
        "s_load_dwordx4 s[12:15], %0, 0x20\n"
        "s_load_dwordx2 s[16:17], %0, 0x30\n"
        "LPC_%=:\n"
        "s_getpc_b64 s[98:99]\n"
        "v_mbcnt_lo_u32_b32 v0, -1, 0\n"
        "v_mbcnt_hi_u32_b32 v0, -1, v0\n"
        "s_mov_b32 s56, s98\n"
        "s_and_b32 s57, s99, 0xffff\n"
        "s_mov_b32 s58, LEND_%=-LPC_%=-4\n"
        "s_mov_b32 s59, 0x20000\n"
        "s_lshl_b32 s92, %1, 10\n"
        "s_and_b32 s92, s92, 0x6000\n"
        "s_cmp_eq_u32 %2, 0\n"
        "s_cselect_b32 s92, s92, 0x100000\n"
        "v_lshlrev_b32 v17, 7, v0\n"
        "buffer_load_dword v17, v17, s[56:59], s92 offen offset:0\n"
        "s_and_b32 s60, %2, 3\n"
        "s_lshr_b32 s22, %2, 2\n"
        "s_lshr_b32 s25, %1, 5\n"
        "s_and_b32 s63, %1, 4\n"
        "s_lshl_b32 s63, s63, 1\n"
        "s_add_u32 s63, s63, s25\n"
        "v_lshrrev_b32 v208, 3, v0\n"
        "v_mul_u32_u24 v243, 0x480, v208\n"
        "v_lshrrev_b32 v209, 4, v0\n"
        "v_and_b32 v210, 7, v0\n"
        "v_xor_b32 v211, v210, v209\n"
        "v_lshl_add_u32 v243, v211, 4, v243\n"
        "v_xor_b32 v15, 64, v243\n"
        "s_lshl_b32 s89, s63, 2\n"
        "s_add_u32 s89, s89, s60\n"
        "s_mul_i32 s89, s89, 36864\n"
        "s_mul_i32 s23, s22, 18432\n"
        "s_add_u32 s23, s23, s89\n"
        "s_lshl_b32 s24, s60, 12\n"
        "s_lshl_b32 s92, s22, 11\n"
        "s_add_u32 s24, s24, s92\n"
        "s_bfe_u32 s62, %1, 0x20003\n"
        "s_lshl_b32 s87, s62, 1\n"
        "s_add_u32 s87, s87, s25\n"
        "s_cmp_ge_u32 s87, 9\n"
        "s_cselect_b32 s92, 9, 0\n"
        "s_sub_u32 s87, s87, s92\n"
        "s_lshl_b32 s87, s87, 7\n"
        "s_waitcnt lgkmcnt(0)\n"
        "s_mov_b64 s[36:37], s[4:5]\n"
        "s_and_b32 s37, s37, 0xffff\n"
        "s_mov_b32 s38, 0x240000\n"
        "s_mov_b32 s39, 0x20000\n"
        "s_add_u32 m0, s24, 0x12000\n"
        "s_add_u32 s94, s87, 0\n"
        "s_cmp_ge_u32 s94, 1152\n"
        "s_cselect_b32 s92, 1152, 0\n"
        "s_sub_u32 s94, s94, s92\n"
        "s_add_u32 s95, s94, s23\n"
        "buffer_load_dwordx4 v243, s[36:39], s95 offen lds\n"
        "s_add_u32 m0, s24, 0x12400\n"
        "s_add_u32 s92, s95, 9216\n"
        "buffer_load_dwordx4 v15, s[36:39], s92 offen lds\n"
        "s_mov_b64 s[40:41], s[6:7]\n"
        "s_and_b32 s41, s41, 0xffff\n"
        "s_mov_b32 s42, 0x510000\n"
        "s_mov_b32 s43, 0x20000\n"
        "s_and_b32 s61, %1, 3\n"
        "s_lshl_b32 s64, s61, 2\n"
        "s_add_u32 s64, s64, s62\n"
        "s_mul_i32 s66, s64, 288\n"
        "s_and_b32 s92, s64, 1\n"
        "s_lshl_b32 s94, s92, 5\n"
        "s_xor_b32 s92, s92, 1\n"
        "s_lshl_b32 s92, s92, 8\n"
        "s_add_u32 s67, s66, s92\n"
        "s_add_u32 s66, s66, s94\n"
        "s_lshl_b32 s66, s66, 1\n"
        "s_lshl_b32 s67, s67, 1\n"
        "s_lshl_b32 s91, %2, 10\n"
        "s_lshl_b32 s92, %2, 3\n"
        "v_add_u32 v212, s92, v208\n"
        "v_and_b32 v211, 1, v209\n"
        "v_lshlrev_b32 v211, 2, v211\n"
        "v_xor_b32 v211, v211, v210\n"
        "v_lshlrev_b32 v211, 4, v211\n"
        "v_mul_u32_u24 v1, 0x2400, v212\n"
        "v_add_u32 v1, v1, v211\n"
        "s_add_u32 s94, s87, 0\n"
        "s_cmp_ge_u32 s94, 1152\n"
        "s_cselect_b32 s92, 1152, 0\n"
        "s_sub_u32 s94, s94, s92\n"
        "s_mul_i32 s94, s94, 4608\n"
        "s_add_u32 s27, s94, s66\n"
        "s_add_u32 m0, s91, 0x0\n"
        "s_nop 0\n"
        "buffer_load_dwordx4 v1, s[40:43], s27 offen lds\n"
        "s_add_u32 m0, s24, 0x16000\n"
        "s_add_u32 s94, s87, 128\n"
        "s_cmp_ge_u32 s94, 1152\n"
        "s_cselect_b32 s92, 1152, 0\n"
        "s_sub_u32 s94, s94, s92\n"
        "s_add_u32 s95, s94, s23\n"
        "buffer_load_dwordx4 v243, s[36:39], s95 offen lds\n"
        "s_add_u32 m0, s24, 0x16400\n"
        "s_add_u32 s92, s95, 9216\n"
        "buffer_load_dwordx4 v15, s[36:39], s92 offen lds\n"
        "s_add_u32 s94, s87, 128\n"
        "s_cmp_ge_u32 s94, 1152\n"
        "s_cselect_b32 s92, 1152, 0\n"
        "s_sub_u32 s94, s94, s92\n"
        "s_mul_i32 s94, s94, 4608\n"
        "s_add_u32 s28, s94, s66\n"
        "s_add_u32 m0, s91, 0x2000\n"
        "s_nop 0\n"
        "buffer_load_dwordx4 v1, s[40:43], s28 offen lds\n"
        "v_mul_u32_u24 v218, 57, v0\n"
        "v_lshrrev_b32 v218, 9, v218\n"
        "v_mul_u32_u24 v219, 9, v218\n"
        "v_sub_u32 v219, v0, v219\n"
        "s_lshl_b32 s93, s25, 3\n"
        "s_add_u32 s93, s93, %2\n"
        "s_mul_i32 s92, s93, 9\n"
        "v_add_u32 v219, s92, v219\n"
        "v_mul_u32_u24 v219, 0x2400, v219\n"
        "v_lshl_add_u32 v244, v218, 7, v219\n"
        "v_add_u32 v244, s66, v244\n"
        "v_add_u32 v219, s67, v219\n"
        "v_cmp_eq_u32 vcc, 4, v218\n"
        "s_nop 1\n"
        "v_cndmask_b32 v244, v244, v219, vcc\n"
        "v_subrev_u32 v18, 0, v0\n"
        "v_cmp_gt_u32 vcc, 0, v18\n"
        "v_mov_b32 v18, 0x7ffffff0\n"
        "s_nop 0\n"
        "v_cndmask_b32 v19, v18, v244, vcc\n"
        "buffer_load_dword v237, v19, s[40:43], 0 offen offset:0\n"
        "s_barrier\n"
        "s_add_u32 s94, s87, 256\n"
        "s_cmp_ge_u32 s94, 1152\n"
        "s_cselect_b32 s92, 1152, 0\n"
        "s_sub_u32 s94, s94, s92\n"
        "s_mul_i32 s94, s94, 4608\n"
        "s_add_u32 s29, s94, s66\n"
        "s_add_u32 s94, s87, 384\n"
        "s_cmp_ge_u32 s94, 1152\n"
        "s_cselect_b32 s92, 1152, 0\n"
        "s_sub_u32 s94, s94, s92\n"
        "s_mul_i32 s94, s94, 4608\n"
        "s_add_u32 s30, s94, s66\n"
        "s_add_u32 s94, s87, 512\n"
        "s_cmp_ge_u32 s94, 1152\n"
        "s_cselect_b32 s92, 1152, 0\n"
        "s_sub_u32 s94, s94, s92\n"
        "s_mul_i32 s94, s94, 4608\n"
        "s_add_u32 s31, s94, s66\n"
        "s_mov_b64 s[48:49], s[16:17]\n"
        "s_and_b32 s49, s49, 0xffff\n"
        "s_mov_b32 s50, 0x2400000\n"
        "s_mov_b32 s51, 0x20000\n"
        "s_lshl_b32 s92, s63, 7\n"
        "s_add_u32 s92, s92, 0x40000\n"
        "v_mov_b32 v22, 0\n"
        "buffer_load_dword v22, v22, s[48:51], s92 offen offset:0 sc1\n"
        "s_lshl_b32 s92, s60, 3\n"
        "v_add_u32 v212, s92, v208\n"
        "v_mul_u32_u24 v4, 0x480, v212\n"
        "v_add_u32 v4, v4, v211\n"
        "v_add_u32 v4, 0x400, v4\n"
        "v_lshlrev_b32 v5, 4, v0\n"
        "v_and_b32 v240, 31, v0\n"
        "v_lshrrev_b32 v241, 5, v0\n"
        "v_bfe_u32 v211, v0, 1, 3\n"
        "v_xor_b32 v211, v211, v241\n"
        "v_lshlrev_b32 v224, 7, v240\n"
        "v_lshl_add_u32 v224, v211, 4, v224\n"
        "s_lshl_b32 s92, s60, 12\n"
        "s_add_u32 s92, s92, 0x12000\n"
        "v_add_u32 v224, s92, v224\n"
        "s_lshl_b32 s26, s22, 1\n"
        "s_xor_b32 s26, s26, s60\n"
        "s_lshl_b32 s92, s26, 5\n"
        "v_xor_b32 v224, s92, v224\n"
        "v_xor_b32 v225, 32, v224\n"
        "v_xor_b32 v226, 64, v224\n"
        "v_xor_b32 v227, 96, v224\n"
        "v_add_u32 v228, 0x10000, v224\n"
        "v_add_u32 v229, 0x10000, v225\n"
        "v_add_u32 v230, 0x10000, v226\n"
        "v_add_u32 v231, 0x10000, v227\n"
        "v_bfe_u32 v211, v0, 2, 2\n"
        "v_and_b32 v213, 3, v0\n"
        "v_lshlrev_b32 v213, 3, v213\n"
        "v_and_b32 v214, 16, v0\n"
        "v_lshl_add_u32 v213, v214, 1, v213\n"
        "v_lshl_add_u32 v212, v241, 3, v211\n"
        "v_lshrrev_b32 v215, 1, v211\n"
        "v_xor_b32 v215, s22, v215\n"
        "v_lshl_add_u32 v215, v215, 6, v213\n"
        "v_lshl_add_u32 v6, v212, 7, v215\n"
        "s_lshl_b32 s92, s26, 11\n"
        "v_add_u32 v6, s92, v6\n"
        "v_xor_b32 v7, 2048, v6\n"
        "v_xor_b32 v8, 4096, v6\n"
        "v_xor_b32 v9, 6144, v6\n"
        "v_add_u32 v10, 0x10000, v6\n"
        "v_add_u32 v11, 0x10000, v7\n"
        "v_add_u32 v12, 0x10000, v8\n"
        "v_add_u32 v13, 0x10000, v9\n"
        "v_lshrrev_b32 v216, 2, v0\n"
        "v_xor_b32 v216, v216, v209\n"
        "v_and_b32 v216, 1, v216\n"
        "v_and_b32 v217, 3, v0\n"
        "v_or_b32 v216, v216, v217\n"
        "v_cmp_eq_u32 vcc, 0, v216\n"
        "v_mov_b32 v216, 0x3f803f80\n"
        "s_nop 0\n"
        "v_cndmask_b32 v232, 0, v216, vcc\n"
        "v_cndmask_b32 v233, 0, v216, vcc\n"
        "v_cndmask_b32 v234, 0, v216, vcc\n"
        "v_cndmask_b32 v235, 0, v216, vcc\n"
        "v_mov_b32 v248, 0\n"
        "v_mov_b32 v249, 0\n"
        "v_mov_b32 v250, 0\n"
        "v_mov_b32 v251, 0\n"
        "v_mov_b32 v242, 0x3f803f80\n"
        "s_add_u32 s85, s87, 640\n"
        "s_cmp_ge_u32 s85, 1152\n"
        "s_cselect_b32 s82, 1152, 0\n"
        "s_sub_u32 s85, s85, s82\n"
        "s_mul_i32 s85, s85, 4608\n"
        "s_add_u32 s68, s85, s66\n"
        "s_add_u32 s85, s87, 768\n"
        "s_cmp_ge_u32 s85, 1152\n"
        "s_cselect_b32 s82, 1152, 0\n"
        "s_sub_u32 s85, s85, s82\n"
        "s_mul_i32 s85, s85, 4608\n"
        "s_add_u32 s69, s85, s66\n"
        "s_add_u32 s85, s87, 896\n"
        "s_cmp_ge_u32 s85, 1152\n"
        "s_cselect_b32 s82, 1152, 0\n"
        "s_sub_u32 s85, s85, s82\n"
        "s_mul_i32 s85, s85, 4608\n"
        "s_add_u32 s20, s85, s66\n"
        "s_add_u32 s85, s87, 1024\n"
        "s_cmp_ge_u32 s85, 1152\n"
        "s_cselect_b32 s82, 1152, 0\n"
        "s_sub_u32 s85, s85, s82\n"
        "s_mul_i32 s85, s85, 4608\n"
        "s_add_u32 s21, s85, s66\n"
        "v_lshrrev_b32 v247, 4, v0\n"
        "v_lshlrev_b32 v24, 2, v240\n"
        "v_add_u32 v24, 0x26000, v24\n"
        "s_lshl_b32 s83, s22, 9\n"
        "v_add_u32 v25, s83, v24\n"
        "v_lshl_add_u32 v25, v241, 8, v25\n"
        "s_lshl_b32 s83, %2, 7\n"
        "v_add_u32 v24, s83, v24\n"
        "s_lshl_b32 s82, s60, 2\n"
        "v_add_u32 v252, s82, v247\n"
        "v_mul_u32_u24 v3, 0x480, v252\n"
        "v_lshlrev_b32 v252, 2, v247\n"
        "v_and_b32 v253, 15, v0\n"
        "v_xor_b32 v252, v252, v253\n"
        "v_lshl_add_u32 v3, v252, 4, v3\n"
        "s_lshl_b32 s82, s62, 8\n"
        "v_add_u32 v3, s82, v3\n"
        "s_lshl_b32 s82, s60, 4\n"
        "v_lshrrev_b32 v252, 2, v0\n"
        "v_add_u32 v252, s82, v252\n"
        "v_mul_u32_u24 v2, 0x2400, v252\n"
        "v_and_b32 v253, 3, v0\n"
        "v_lshl_add_u32 v2, v253, 4, v2\n"
        "s_mov_b64 s[56:57], s[8:9]\n"
        "s_and_b32 s57, s57, 0xffff\n"
        "s_mov_b32 s58, 0x2400\n"
        "s_mov_b32 s59, 0x20000\n"
        "s_lshl_b32 s83, s25, 3\n"
        "s_add_u32 s83, s83, %2\n"
        "s_mul_i32 s83, s83, 9\n"
        "s_lshr_b32 s83, s83, 1\n"
        "v_mul_u32_u24 v252, 103, v0\n"
        "v_lshrrev_b32 v252, 10, v252\n"
        "v_mul_u32_u24 v253, 10, v252\n"
        "v_sub_u32 v253, v0, v253\n"
        "v_and_b32 v252, 3, v252\n"
        "v_xor_b32 v252, s64, v252\n"
        "v_mul_u32_u24 v252, 0x120, v252\n"
        "v_add_u32 v252, s83, v252\n"
        "v_add_u32 v254, v252, v253\n"
        "v_mul_u32_u24 v254, 0x480, v254\n"
        "v_add_u32 v254, 0x400, v254\n"
        "v_lshrrev_b32 v14, 1, v253\n"
        "v_add_u32 v14, v252, v14\n"
        "v_mul_u32_u24 v14, 0x480, v14\n"
        "v_and_b32 v253, 1, v253\n"
        "v_lshl_add_u32 v14, v253, 7, v14\n"
        "s_lshl_b32 s82, s62, 8\n"
        "v_add_u32 v14, s82, v14\n"
        "v_cmp_lt_u32 vcc, 39, v0\n"
        "s_nop 1\n"
        "v_cndmask_b32 v14, v14, v254, vcc\n"
        "s_xor_b32 s84, s64, 0\n"
        "s_mul_i32 s70, s84, 288\n"
        "s_and_b32 s82, s84, 1\n"
        "s_lshl_b32 s85, s82, 5\n"
        "s_xor_b32 s82, s82, 1\n"
        "s_lshl_b32 s82, s82, 8\n"
        "s_add_u32 s74, s70, s82\n"
        "s_add_u32 s70, s70, s85\n"
        "s_lshl_b32 s82, s22, 5\n"
        "s_add_u32 s70, s70, s82\n"
        "s_mul_i32 s70, s70, 1152\n"
        "s_lshl_b32 s82, s22, 4\n"
        "s_add_u32 s74, s74, s82\n"
        "s_mul_i32 s74, s74, 1152\n"
        "s_lshl_b32 s82, s63, 4\n"
        "s_add_u32 s82, s82, s84\n"
        "s_lshl_b32 s82, s82, 3\n"
        "s_add_u32 s82, s82, %2\n"
        "s_mul_i32 s82, s82, 9216\n"
        "s_add_u32 s78, s82, 0x200000\n"
        "s_sub_u32 s88, s67, s66\n"
        "s_lshl_b32 s82, s63, 14\n"
        "s_lshl_b32 s83, %2, 7\n"
        "s_add_u32 s82, s82, s83\n"
        "v_and_b32 v23, 3, v0\n"
        "v_xor_b32 v23, s64, v23\n"
        "v_lshlrev_b32 v23, 10, v23\n"
        "v_add_u32 v23, s82, v23\n"
        "s_mov_b64 s[44:45], s[10:11]\n"
        "s_and_b32 s45, s45, 0xffff\n"
        "s_mov_b32 s46, 0x510000\n"
        "s_mov_b32 s47, 0x20000\n"
        "s_mov_b64 s[52:53], s[14:15]\n"
        "s_and_b32 s53, s53, 0xffff\n"
        "s_mov_b32 s54, 0x240000\n"
        "s_mov_b32 s55, 0x20000\n"
        "s_xor_b32 s84, s64, 1\n"
        "s_mul_i32 s71, s84, 288\n"
        "s_and_b32 s82, s84, 1\n"
        "s_lshl_b32 s85, s82, 5\n"
        "s_xor_b32 s82, s82, 1\n"
        "s_lshl_b32 s82, s82, 8\n"
        "s_add_u32 s75, s71, s82\n"
        "s_add_u32 s71, s71, s85\n"
        "s_lshl_b32 s82, s22, 5\n"
        "s_add_u32 s71, s71, s82\n"
        "s_mul_i32 s71, s71, 1152\n"
        "s_lshl_b32 s82, s22, 4\n"
        "s_add_u32 s75, s75, s82\n"
        "s_mul_i32 s75, s75, 1152\n"
        "s_lshl_b32 s82, s63, 4\n"
        "s_add_u32 s82, s82, s84\n"
        "s_lshl_b32 s82, s82, 3\n"
        "s_add_u32 s82, s82, %2\n"
        "s_mul_i32 s82, s82, 9216\n"
        "s_add_u32 s79, s82, 0x200000\n"
        "s_xor_b32 s84, s64, 2\n"
        "s_mul_i32 s72, s84, 288\n"
        "s_and_b32 s82, s84, 1\n"
        "s_lshl_b32 s85, s82, 5\n"
        "s_xor_b32 s82, s82, 1\n"
        "s_lshl_b32 s82, s82, 8\n"
        "s_add_u32 s76, s72, s82\n"
        "s_add_u32 s72, s72, s85\n"
        "s_lshl_b32 s82, s22, 5\n"
        "s_add_u32 s72, s72, s82\n"
        "s_mul_i32 s72, s72, 1152\n"
        "s_lshl_b32 s82, s22, 4\n"
        "s_add_u32 s76, s76, s82\n"
        "s_mul_i32 s76, s76, 1152\n"
        "s_lshl_b32 s82, s63, 4\n"
        "s_add_u32 s82, s82, s84\n"
        "s_lshl_b32 s82, s82, 3\n"
        "s_add_u32 s82, s82, %2\n"
        "s_mul_i32 s82, s82, 9216\n"
        "s_add_u32 s80, s82, 0x200000\n"
        "s_xor_b32 s84, s64, 3\n"
        "s_mul_i32 s73, s84, 288\n"
        "s_and_b32 s82, s84, 1\n"
        "s_lshl_b32 s85, s82, 5\n"
        "s_xor_b32 s82, s82, 1\n"
        "s_lshl_b32 s82, s82, 8\n"
        "s_add_u32 s77, s73, s82\n"
        "s_add_u32 s73, s73, s85\n"
        "s_lshl_b32 s82, s22, 5\n"
        "s_add_u32 s73, s73, s82\n"
        "s_mul_i32 s73, s73, 1152\n"
        "s_lshl_b32 s82, s22, 4\n"
        "s_add_u32 s77, s77, s82\n"
        "s_mul_i32 s77, s77, 1152\n"
        "s_lshl_b32 s82, s63, 4\n"
        "s_add_u32 s82, s82, s84\n"
        "s_lshl_b32 s82, s82, 3\n"
        "s_add_u32 s82, s82, %2\n"
        "s_mul_i32 s82, s82, 9216\n"
        "s_add_u32 s81, s82, 0x200000\n"
        "s_add_u32 m0, s24, 0x1a000\n"
        "s_add_u32 s94, s87, 256\n"
        "s_cmp_ge_u32 s94, 1152\n"
        "s_cselect_b32 s92, 1152, 0\n"
        "s_sub_u32 s94, s94, s92\n"
        "s_add_u32 s95, s94, s23\n"
        "buffer_load_dwordx4 v243, s[36:39], s95 offen lds\n"
        "s_add_u32 m0, s24, 0x1a400\n"
        "s_add_u32 s92, s95, 9216\n"
        "buffer_load_dwordx4 v15, s[36:39], s92 offen lds\n"
        "s_add_u32 m0, s91, 0x4000\n"
        "s_nop 0\n"
        "buffer_load_dwordx4 v1, s[40:43], s29 offen lds\n"
        "s_add_u32 m0, s24, 0x1e000\n"
        "s_add_u32 s94, s87, 384\n"
        "s_cmp_ge_u32 s94, 1152\n"
        "s_cselect_b32 s92, 1152, 0\n"
        "s_sub_u32 s94, s94, s92\n"
        "s_add_u32 s95, s94, s23\n"
        "buffer_load_dwordx4 v243, s[36:39], s95 offen lds\n"
        "s_add_u32 m0, s24, 0x1e400\n"
        "s_add_u32 s92, s95, 9216\n"
        "buffer_load_dwordx4 v15, s[36:39], s92 offen lds\n"
        "s_add_u32 m0, s91, 0x6000\n"
        "s_nop 0\n"
        "buffer_load_dwordx4 v1, s[40:43], s30 offen lds\n"
        "s_waitcnt vmcnt(12)\n"
        "s_waitcnt vmcnt(11)\n"
        "s_barrier\n"
        "ds_read_b128 v[48:51], v224 offset:0\n"
        "ds_read_b128 v[52:55], v225 offset:0\n"
        "ds_read_b128 v[56:59], v226 offset:0\n"
        "ds_read_b128 v[60:63], v227 offset:0\n"
        "ds_read_b64_tr_b16 v[28:29], v6 offset:0\n"
        "ds_read_b64_tr_b16 v[30:31], v6 offset:512\n"
        "ds_read_b64_tr_b16 v[32:33], v7 offset:0\n"
        "ds_read_b64_tr_b16 v[34:35], v7 offset:512\n"
        "ds_read_b64_tr_b16 v[36:37], v8 offset:0\n"
        "ds_read_b64_tr_b16 v[38:39], v8 offset:512\n"
        "ds_read_b64_tr_b16 v[40:41], v9 offset:0\n"
        "ds_read_b64_tr_b16 v[42:43], v9 offset:512\n"
        "s_add_u32 m0, s24, 0x22000\n"
        "s_add_u32 s94, s87, 512\n"
        "s_cmp_ge_u32 s94, 1152\n"
        "s_cselect_b32 s92, 1152, 0\n"
        "s_sub_u32 s94, s94, s92\n"
        "s_add_u32 s95, s94, s23\n"
        "buffer_load_dwordx4 v243, s[36:39], s95 offen lds\n"
        "ds_read_b128 v[48:51], v224 offset:0\n"
        "s_add_u32 m0, s24, 0x22400\n"
        "s_add_u32 s92, s95, 9216\n"
        "buffer_load_dwordx4 v15, s[36:39], s92 offen lds\n"
        "ds_read_b128 v[52:55], v225 offset:0\n"
        "s_add_u32 m0, s91, 0x8000\n"
        "s_nop 0\n"
        "buffer_load_dwordx4 v1, s[40:43], s31 offen lds\n"
        "ds_read_b128 v[56:59], v226 offset:0\n"
        "s_add_u32 m0, s91, 0xa000\n"
        "s_nop 0\n"
        "buffer_load_dwordx4 v1, s[40:43], s68 offen lds\n"
        "ds_read_b128 v[60:63], v227 offset:0\n"
        "s_add_u32 m0, s91, 0xc000\n"
        "s_nop 0\n"
        "buffer_load_dwordx4 v1, s[40:43], s69 offen lds\n"
        "s_waitcnt vmcnt(14)\n"
        "s_waitcnt lgkmcnt(0)\n"
        "s_waitcnt vmcnt(13)\n"
        "s_barrier\n"
        "v_dot2c_f32_bf16 v248, v48, v242\n"
        "v_dot2c_f32_bf16 v250, v48, v48\n"
        "v_dot2c_f32_bf16 v249, v49, v242\n"
        "v_dot2c_f32_bf16 v251, v49, v49\n"
        "v_dot2c_f32_bf16 v248, v50, v242\n"
        "v_dot2c_f32_bf16 v250, v50, v50\n"
        "v_dot2c_f32_bf16 v249, v51, v242\n"
        "v_dot2c_f32_bf16 v251, v51, v51\n"
        "v_dot2c_f32_bf16 v248, v52, v242\n"
        "v_dot2c_f32_bf16 v250, v52, v52\n"
        "v_dot2c_f32_bf16 v249, v53, v242\n"
        "v_dot2c_f32_bf16 v251, v53, v53\n"
        "v_dot2c_f32_bf16 v248, v54, v242\n"
        "v_dot2c_f32_bf16 v250, v54, v54\n"
        "v_dot2c_f32_bf16 v249, v55, v242\n"
        "v_dot2c_f32_bf16 v251, v55, v55\n"
        "s_setprio 3\n"
        "s_add_u32 m0, s24, 0x12000\n"
        "s_add_u32 s94, s87, 640\n"
        "s_cmp_ge_u32 s94, 1152\n"
        "s_cselect_b32 s92, 1152, 0\n"
        "s_sub_u32 s94, s94, s92\n"
        "s_add_u32 s95, s94, s23\n"
        "buffer_load_dwordx4 v243, s[36:39], s95 offen lds\n"
        "v_mfma_f32_32x32x16_bf16 v[192:207], v[28:31], v[48:51], 0\n"
        "ds_read_b64_tr_b16 v[44:45], v6 offset:8192\n"
        "ds_read_b64_tr_b16 v[46:47], v6 offset:8704\n"
        "s_setprio 2\n"
        "v_mfma_f32_16x16x32_bf16 v[236:239], v[232:235], v[28:31], 0\n"
        "s_add_u32 m0, s24, 0x12400\n"
        "s_add_u32 s92, s95, 9216\n"
        "buffer_load_dwordx4 v15, s[36:39], s92 offen lds\n"
        "v_mfma_f32_32x32x16_bf16 v[192:207], v[32:35], v[52:55], v[192:207]\n"
        "ds_read_b64_tr_b16 v[28:29], v7 offset:8192\n"
        "ds_read_b64_tr_b16 v[30:31], v7 offset:8704\n"
        "s_setprio 1\n"
        "s_add_u32 m0, s91, 0xe000\n"
        "ds_read_b128 v[64:67], v224 offset:16384\n"
        "ds_read_b128 v[68:71], v225 offset:16384\n"
        "buffer_load_dwordx4 v1, s[40:43], s20 offen lds\n"
        "v_mfma_f32_32x32x16_bf16 v[192:207], v[36:39], v[56:59], v[192:207]\n"
        "ds_read_b64_tr_b16 v[32:33], v8 offset:8192\n"
        "ds_read_b64_tr_b16 v[34:35], v8 offset:8704\n"
        "s_setprio 0\n"
        "s_add_u32 m0, s91, 0x10000\n"
        "ds_read_b128 v[72:75], v226 offset:16384\n"
        "ds_read_b128 v[76:79], v227 offset:16384\n"
        "buffer_load_dwordx4 v1, s[40:43], s21 offen lds\n"
        "v_mfma_f32_32x32x16_bf16 v[192:207], v[40:43], v[60:63], v[192:207]\n"
        "ds_read_b64_tr_b16 v[36:37], v9 offset:8192\n"
        "ds_read_b64_tr_b16 v[38:39], v9 offset:8704\n"
        "s_waitcnt vmcnt(10)\n"
        "s_waitcnt lgkmcnt(2)\n"
        "s_barrier\n"
        "v_dot2c_f32_bf16 v248, v64, v242\n"
        "v_dot2c_f32_bf16 v250, v64, v64\n"
        "v_dot2c_f32_bf16 v249, v65, v242\n"
        "v_dot2c_f32_bf16 v251, v65, v65\n"
        "v_dot2c_f32_bf16 v248, v66, v242\n"
        "v_dot2c_f32_bf16 v250, v66, v66\n"
        "v_dot2c_f32_bf16 v249, v67, v242\n"
        "v_dot2c_f32_bf16 v251, v67, v67\n"
        "v_dot2c_f32_bf16 v248, v68, v242\n"
        "v_dot2c_f32_bf16 v250, v68, v68\n"
        "v_dot2c_f32_bf16 v249, v69, v242\n"
        "v_dot2c_f32_bf16 v251, v69, v69\n"
        "v_dot2c_f32_bf16 v248, v70, v242\n"
        "v_dot2c_f32_bf16 v250, v70, v70\n"
        "v_dot2c_f32_bf16 v249, v71, v242\n"
        "v_dot2c_f32_bf16 v251, v71, v71\n"
        "s_setprio 3\n"
        "s_add_u32 m0, s24, 0x16000\n"
        "s_add_u32 s94, s87, 768\n"
        "s_cmp_ge_u32 s94, 1152\n"
        "s_cselect_b32 s92, 1152, 0\n"
        "s_sub_u32 s94, s94, s92\n"
        "s_add_u32 s95, s94, s23\n"
        "buffer_load_dwordx4 v243, s[36:39], s95 offen lds\n"
        "v_mfma_f32_32x32x16_bf16 v[192:207], v[44:47], v[64:67], v[192:207]\n"
        "ds_read_b64_tr_b16 v[40:41], v6 offset:16384\n"
        "ds_read_b64_tr_b16 v[42:43], v6 offset:16896\n"
        "s_setprio 2\n"
        "v_mfma_f32_16x16x32_bf16 v[236:239], v[232:235], v[44:47], v[236:239]\n"
        "v_mfma_f32_32x32x16_bf16 v[192:207], v[28:31], v[68:71], v[192:207]\n"
        "ds_read_b64_tr_b16 v[44:45], v7 offset:16384\n"
        "ds_read_b64_tr_b16 v[46:47], v7 offset:16896\n"
        "s_setprio 1\n"
        "ds_read_b128 v[80:83], v224 offset:32768\n"
        "ds_read_b128 v[84:87], v225 offset:32768\n"
        "ds_read_b128 v[88:91], v226 offset:32768\n"
        "s_add_u32 m0, s24, 0x16400\n"
        "s_add_u32 s92, s95, 9216\n"
        "buffer_load_dwordx4 v15, s[36:39], s92 offen lds\n"
        "v_mfma_f32_32x32x16_bf16 v[192:207], v[32:35], v[72:75], v[192:207]\n"
        "ds_read_b64_tr_b16 v[28:29], v8 offset:16384\n"
        "ds_read_b64_tr_b16 v[30:31], v8 offset:16896\n"
        "s_setprio 0\n"
        "ds_read_b128 v[92:95], v227 offset:32768\n"
        "ds_read_b128 v[96:99], v224 offset:49152\n"
        "ds_read_b128 v[100:103], v225 offset:49152\n"
        "ds_read_b128 v[104:107], v226 offset:49152\n"
        "s_add_u32 m0, s91, 0x0\n"
        "s_add_u32 s92, s27, 0x80\n"
        "buffer_load_dwordx4 v1, s[40:43], s92 offen lds\n"
        "s_waitcnt lgkmcnt(11)\n"
        "v_mfma_f32_32x32x16_bf16 v[192:207], v[36:39], v[76:79], v[192:207]\n"
        "ds_read_b64_tr_b16 v[32:33], v9 offset:16384\n"
        "ds_read_b64_tr_b16 v[34:35], v9 offset:16896\n"
        "ds_read_b128 v[108:111], v227 offset:49152\n"
        "s_waitcnt vmcnt(5)\n"
        "s_waitcnt lgkmcnt(0)\n"
        "s_barrier\n"
        "v_dot2c_f32_bf16 v248, v80, v242\n"
        "v_dot2c_f32_bf16 v250, v80, v80\n"
        "v_dot2c_f32_bf16 v249, v81, v242\n"
        "v_dot2c_f32_bf16 v251, v81, v81\n"
        "v_dot2c_f32_bf16 v248, v82, v242\n"
        "v_dot2c_f32_bf16 v250, v82, v82\n"
        "v_dot2c_f32_bf16 v249, v83, v242\n"
        "v_dot2c_f32_bf16 v251, v83, v83\n"
        "v_dot2c_f32_bf16 v248, v84, v242\n"
        "v_dot2c_f32_bf16 v250, v84, v84\n"
        "v_dot2c_f32_bf16 v249, v85, v242\n"
        "v_dot2c_f32_bf16 v251, v85, v85\n"
        "v_dot2c_f32_bf16 v248, v86, v242\n"
        "v_dot2c_f32_bf16 v250, v86, v86\n"
        "v_dot2c_f32_bf16 v249, v87, v242\n"
        "v_dot2c_f32_bf16 v251, v87, v87\n"
        "v_dot2c_f32_bf16 v248, v96, v242\n"
        "v_dot2c_f32_bf16 v250, v96, v96\n"
        "v_dot2c_f32_bf16 v249, v97, v242\n"
        "v_dot2c_f32_bf16 v251, v97, v97\n"
        "v_dot2c_f32_bf16 v248, v98, v242\n"
        "v_dot2c_f32_bf16 v250, v98, v98\n"
        "v_dot2c_f32_bf16 v249, v99, v242\n"
        "v_dot2c_f32_bf16 v251, v99, v99\n"
        "v_dot2c_f32_bf16 v248, v100, v242\n"
        "v_dot2c_f32_bf16 v250, v100, v100\n"
        "v_dot2c_f32_bf16 v249, v101, v242\n"
        "v_dot2c_f32_bf16 v251, v101, v101\n"
        "v_dot2c_f32_bf16 v248, v102, v242\n"
        "v_dot2c_f32_bf16 v250, v102, v102\n"
        "v_dot2c_f32_bf16 v249, v103, v242\n"
        "v_dot2c_f32_bf16 v251, v103, v103\n"
        "s_setprio 3\n"
        "s_add_u32 m0, s24, 0x1a000\n"
        "s_add_u32 s94, s87, 896\n"
        "s_cmp_ge_u32 s94, 1152\n"
        "s_cselect_b32 s92, 1152, 0\n"
        "s_sub_u32 s94, s94, s92\n"
        "s_add_u32 s95, s94, s23\n"
        "buffer_load_dwordx4 v243, s[36:39], s95 offen lds\n"
        "v_mfma_f32_32x32x16_bf16 v[192:207], v[40:43], v[80:83], v[192:207]\n"
        "ds_read_b64_tr_b16 v[36:37], v6 offset:24576\n"
        "ds_read_b64_tr_b16 v[38:39], v6 offset:25088\n"
        "v_mfma_f32_16x16x32_bf16 v[236:239], v[232:235], v[40:43], v[236:239]\n"
        "v_mfma_f32_32x32x16_bf16 v[192:207], v[44:47], v[84:87], v[192:207]\n"
        "ds_read_b64_tr_b16 v[40:41], v7 offset:24576\n"
        "ds_read_b64_tr_b16 v[42:43], v7 offset:25088\n"
        "s_setprio 2\n"
        "s_add_u32 m0, s24, 0x1a400\n"
        "s_add_u32 s92, s95, 9216\n"
        "buffer_load_dwordx4 v15, s[36:39], s92 offen lds\n"
        "v_mfma_f32_32x32x16_bf16 v[192:207], v[28:31], v[88:91], v[192:207]\n"
        "ds_read_b64_tr_b16 v[44:45], v8 offset:24576\n"
        "ds_read_b64_tr_b16 v[46:47], v8 offset:25088\n"
        "ds_read_b128 v[112:115], v228 offset:0\n"
        "ds_read_b128 v[116:119], v229 offset:0\n"
        "v_mfma_f32_32x32x16_bf16 v[192:207], v[32:35], v[92:95], v[192:207]\n"
        "ds_read_b64_tr_b16 v[28:29], v9 offset:24576\n"
        "ds_read_b64_tr_b16 v[30:31], v9 offset:25088\n"
        "s_setprio 1\n"
        "s_add_u32 m0, s91, 0x1f80\n"
        "ds_read_b128 v[120:123], v230 offset:0\n"
        "buffer_load_dwordx4 v1, s[40:43], s28 offen offset:128 lds\n"
        "s_waitcnt lgkmcnt(7)\n"
        "v_mfma_f32_32x32x16_bf16 v[192:207], v[36:39], v[96:99], v[192:207]\n"
        "ds_read_b64_tr_b16 v[32:33], v6 offset:32768\n"
        "ds_read_b64_tr_b16 v[34:35], v6 offset:33280\n"
        "ds_read_b128 v[124:127], v231 offset:0\n"
        "ds_read_b128 v[128:131], v224 offset:0\n"
        "v_mfma_f32_16x16x32_bf16 v[236:239], v[232:235], v[36:39], v[236:239]\n"
        "s_add_u32 m0, s24, 0x1e000\n"
        "s_add_u32 s94, s87, 1024\n"
        "s_cmp_ge_u32 s94, 1152\n"
        "s_cselect_b32 s92, 1152, 0\n"
        "s_sub_u32 s94, s94, s92\n"
        "s_add_u32 s95, s94, s23\n"
        "buffer_load_dwordx4 v243, s[36:39], s95 offen lds\n"
        "v_mfma_f32_32x32x16_bf16 v[192:207], v[40:43], v[100:103], v[192:207]\n"
        "ds_read_b64_tr_b16 v[36:37], v7 offset:32768\n"
        "ds_read_b64_tr_b16 v[38:39], v7 offset:33280\n"
        "s_setprio 0\n"
        "ds_read_b128 v[132:135], v225 offset:0\n"
        "ds_read_b128 v[136:139], v226 offset:0\n"
        "v_subrev_u32 v18, 0, v0\n"
        "v_cmp_gt_u32 vcc, 10, v18\n"
        "v_mov_b32 v18, 0x7ffffff0\n"
        "s_nop 0\n"
        "v_cndmask_b32 v19, v18, v14, vcc\n"
        "buffer_load_dword v237, v19, s[44:47], 0 offen offset:0\n"
        "s_waitcnt lgkmcnt(9)\n"
        "v_mfma_f32_32x32x16_bf16 v[192:207], v[44:47], v[104:107], v[192:207]\n"
        "ds_read_b64_tr_b16 v[40:41], v8 offset:32768\n"
        "ds_read_b64_tr_b16 v[42:43], v8 offset:33280\n"
        "ds_read_b128 v[140:143], v227 offset:0\n"
        "s_add_u32 m0, s24, 0x1e400\n"
        "s_add_u32 s92, s95, 9216\n"
        "buffer_load_dwordx4 v15, s[36:39], s92 offen lds\n"
        "v_mfma_f32_32x32x16_bf16 v[192:207], v[28:31], v[108:111], v[192:207]\n"
        "ds_read_b64_tr_b16 v[44:45], v9 offset:32768\n"
        "ds_read_b64_tr_b16 v[46:47], v9 offset:33280\n"
        "v_readfirstlane_b32 s65, v22\n"
        "s_nop 0\n"
        "s_mul_i32 s65, s65, 0x41c64e6d\n"
        "s_add_u32 s65, s65, 0x9e3779b9\n"
        "s_lshl_b32 s92, s63, 14\n"
        "s_lshl_b32 s93, %2, 7\n"
        "s_add_u32 s92, s92, s93\n"
        "s_lshl_b32 s93, s64, 10\n"
        "s_add_u32 s92, s92, s93\n"
        "v_mov_b32 v22, 0x7ffffff0\n"
        "v_writelane_b32 v22, s92, 0\n"
        "s_waitcnt vmcnt(7)\n"
        "s_waitcnt lgkmcnt(2)\n"
        "s_barrier\n"
        "v_dot2c_f32_bf16 v248, v112, v242\n"
        "v_dot2c_f32_bf16 v250, v112, v112\n"
        "v_dot2c_f32_bf16 v249, v113, v242\n"
        "v_dot2c_f32_bf16 v251, v113, v113\n"
        "v_dot2c_f32_bf16 v248, v114, v242\n"
        "v_dot2c_f32_bf16 v250, v114, v114\n"
        "v_dot2c_f32_bf16 v249, v115, v242\n"
        "v_dot2c_f32_bf16 v251, v115, v115\n"
        "v_dot2c_f32_bf16 v248, v116, v242\n"
        "v_dot2c_f32_bf16 v250, v116, v116\n"
        "v_dot2c_f32_bf16 v249, v117, v242\n"
        "v_dot2c_f32_bf16 v251, v117, v117\n"
        "v_dot2c_f32_bf16 v248, v118, v242\n"
        "v_dot2c_f32_bf16 v250, v118, v118\n"
        "v_dot2c_f32_bf16 v249, v119, v242\n"
        "v_dot2c_f32_bf16 v251, v119, v119\n"
        "v_dot2c_f32_bf16 v248, v128, v242\n"
        "v_dot2c_f32_bf16 v250, v128, v128\n"
        "v_dot2c_f32_bf16 v249, v129, v242\n"
        "v_dot2c_f32_bf16 v251, v129, v129\n"
        "v_dot2c_f32_bf16 v248, v130, v242\n"
        "v_dot2c_f32_bf16 v250, v130, v130\n"
        "v_dot2c_f32_bf16 v249, v131, v242\n"
        "v_dot2c_f32_bf16 v251, v131, v131\n"
        "v_dot2c_f32_bf16 v248, v132, v242\n"
        "v_dot2c_f32_bf16 v250, v132, v132\n"
        "v_dot2c_f32_bf16 v249, v133, v242\n"
        "v_dot2c_f32_bf16 v251, v133, v133\n"
        "v_dot2c_f32_bf16 v248, v134, v242\n"
        "v_dot2c_f32_bf16 v250, v134, v134\n"
        "v_dot2c_f32_bf16 v249, v135, v242\n"
        "v_dot2c_f32_bf16 v251, v135, v135\n"
        "s_setprio 3\n"
        "s_add_u32 m0, s91, 0x3f80\n"
        "s_nop 0\n"
        "buffer_load_dwordx4 v1, s[40:43], s29 offen offset:128 lds\n"
        "v_mfma_f32_32x32x16_bf16 v[192:207], v[32:35], v[112:115], v[192:207]\n"
        "ds_read_b64_tr_b16 v[28:29], v6 offset:40960\n"
        "ds_read_b64_tr_b16 v[30:31], v6 offset:41472\n"
        "v_mfma_f32_16x16x32_bf16 v[236:239], v[232:235], v[32:35], v[236:239]\n"
        "v_mfma_f32_32x32x16_bf16 v[192:207], v[36:39], v[116:119], v[192:207]\n"
        "ds_read_b64_tr_b16 v[32:33], v7 offset:40960\n"
        "ds_read_b64_tr_b16 v[34:35], v7 offset:41472\n"
        "s_setprio 2\n"
        "s_add_u32 m0, s91, 0x5f80\n"
        "s_nop 0\n"
        "buffer_load_dwordx4 v1, s[40:43], s30 offen offset:128 lds\n"
        "v_mfma_f32_32x32x16_bf16 v[192:207], v[40:43], v[120:123], v[192:207]\n"
        "ds_read_b64_tr_b16 v[36:37], v8 offset:40960\n"
        "ds_read_b64_tr_b16 v[38:39], v8 offset:41472\n"
        "ds_read_b128 v[144:147], v224 offset:16384\n"
        "s_waitcnt lgkmcnt(5)\n"
        "v_mfma_f32_32x32x16_bf16 v[192:207], v[44:47], v[124:127], v[192:207]\n"
        "ds_read_b64_tr_b16 v[40:41], v9 offset:40960\n"
        "ds_read_b64_tr_b16 v[42:43], v9 offset:41472\n"
        "s_setprio 1\n"
        "s_add_u32 m0, s91, 0x11f80\n"
        "ds_read_b128 v[148:151], v225 offset:16384\n"
        "buffer_load_dwordx4 v1, s[40:43], s31 offen offset:128 lds\n"
        "v_mfma_f32_32x32x16_bf16 v[192:207], v[28:31], v[128:131], v[192:207]\n"
        "ds_read_b64_tr_b16 v[44:45], v6 offset:49152\n"
        "ds_read_b64_tr_b16 v[46:47], v6 offset:49664\n"
        "ds_read_b128 v[152:155], v226 offset:16384\n"
        "v_mfma_f32_16x16x32_bf16 v[236:239], v[232:235], v[28:31], v[236:239]\n"
        "s_waitcnt lgkmcnt(7)\n"
        "v_mfma_f32_32x32x16_bf16 v[192:207], v[32:35], v[132:135], v[192:207]\n"
        "ds_read_b64_tr_b16 v[28:29], v7 offset:49152\n"
        "ds_read_b64_tr_b16 v[30:31], v7 offset:49664\n"
        "s_setprio 0\n"
        "s_add_u32 m0, s91, 0x13f80\n"
        "ds_read_b128 v[156:159], v227 offset:16384\n"
        "v_subrev_u32 v18, 40, v0\n"
        "v_cmp_gt_u32 vcc, 5, v18\n"
        "v_mov_b32 v18, 0x7ffffff0\n"
        "s_nop 0\n"
        "v_cndmask_b32 v19, v18, v14, vcc\n"
        "buffer_load_dword v237, v19, s[44:47], 0 offen offset:0\n"
        "buffer_load_dwordx4 v1, s[40:43], s68 offen offset:128 lds\n"
        "v_mfma_f32_32x32x16_bf16 v[192:207], v[36:39], v[136:139], v[192:207]\n"
        "ds_read_b64_tr_b16 v[32:33], v8 offset:49152\n"
        "ds_read_b64_tr_b16 v[34:35], v8 offset:49664\n"
        "s_waitcnt lgkmcnt(6)\n"
        "v_mfma_f32_32x32x16_bf16 v[192:207], v[40:43], v[140:143], v[192:207]\n"
        "ds_read_b64_tr_b16 v[36:37], v9 offset:49152\n"
        "ds_read_b64_tr_b16 v[38:39], v9 offset:49664\n"
        "s_waitcnt vmcnt(5)\n"
        "s_waitcnt lgkmcnt(4)\n"
        "s_barrier\n"
        "v_dot2c_f32_bf16 v248, v144, v242\n"
        "v_dot2c_f32_bf16 v250, v144, v144\n"
        "v_dot2c_f32_bf16 v249, v145, v242\n"
        "v_dot2c_f32_bf16 v251, v145, v145\n"
        "v_dot2c_f32_bf16 v248, v146, v242\n"
        "v_dot2c_f32_bf16 v250, v146, v146\n"
        "v_dot2c_f32_bf16 v249, v147, v242\n"
        "v_dot2c_f32_bf16 v251, v147, v147\n"
        "v_dot2c_f32_bf16 v248, v148, v242\n"
        "v_dot2c_f32_bf16 v250, v148, v148\n"
        "v_dot2c_f32_bf16 v249, v149, v242\n"
        "v_dot2c_f32_bf16 v251, v149, v149\n"
        "v_dot2c_f32_bf16 v248, v150, v242\n"
        "v_dot2c_f32_bf16 v250, v150, v150\n"
        "v_dot2c_f32_bf16 v249, v151, v242\n"
        "v_dot2c_f32_bf16 v251, v151, v151\n"
        "s_setprio 3\n"
        "s_add_u32 m0, s91, 0x7f80\n"
        "ds_read_b128 v[160:163], v224 offset:32768\n"
        "buffer_load_dwordx4 v1, s[40:43], s69 offen offset:128 lds\n"
        "v_mfma_f32_32x32x16_bf16 v[192:207], v[44:47], v[144:147], v[192:207]\n"
        "ds_read_b64_tr_b16 v[40:41], v6 offset:57344\n"
        "ds_read_b64_tr_b16 v[42:43], v6 offset:57856\n"
        "ds_read_b128 v[164:167], v225 offset:32768\n"
        "ds_read_b128 v[168:171], v226 offset:32768\n"
        "v_mfma_f32_16x16x32_bf16 v[236:239], v[232:235], v[44:47], v[236:239]\n"
        "v_mfma_f32_32x32x16_bf16 v[192:207], v[28:31], v[148:151], v[192:207]\n"
        "ds_read_b64_tr_b16 v[44:45], v7 offset:57344\n"
        "ds_read_b64_tr_b16 v[46:47], v7 offset:57856\n"
        "v_lshlrev_b32 v26, 25, v0\n"
        "v_and_b32 v26, 0x40000000, v26\n"
        "v_lshl_add_u32 v26, v0, 1, v26\n"
        "s_lshl_b32 s93, s22, 6\n"
        "s_add_u32 s93, s93, s66\n"
        "buffer_load_ushort v26, v26, s[56:59], s93 offen offset:0\n"
        "ds_read_b128 v[172:175], v227 offset:32768\n"
        "s_waitcnt lgkmcnt(8)\n"
        "v_mfma_f32_32x32x16_bf16 v[192:207], v[32:35], v[152:155], v[192:207]\n"
        "ds_read_b64_tr_b16 v[28:29], v8 offset:57344\n"
        "ds_read_b64_tr_b16 v[30:31], v8 offset:57856\n"
        "s_setprio 2\n"
        "s_add_u32 m0, s91, 0x9f80\n"
        "ds_read_b128 v[176:179], v224 offset:49152\n"
        "buffer_load_dwordx4 v1, s[40:43], s20 offen offset:128 lds\n"
        "v_mfma_f32_32x32x16_bf16 v[192:207], v[36:39], v[156:159], v[192:207]\n"
        "ds_read_b64_tr_b16 v[32:33], v9 offset:57344\n"
        "ds_read_b64_tr_b16 v[34:35], v9 offset:57856\n"
        "ds_read_b128 v[180:183], v225 offset:49152\n"
        "s_waitcnt lgkmcnt(7)\n"
        "v_mfma_f32_32x32x16_bf16 v[192:207], v[40:43], v[160:163], v[192:207]\n"
        "ds_read_b64_tr_b16 v[36:37], v10 offset:0\n"
        "ds_read_b64_tr_b16 v[38:39], v10 offset:512\n"
        "ds_read_b128 v[184:187], v226 offset:49152\n"
        "v_mfma_f32_16x16x32_bf16 v[236:239], v[232:235], v[40:43], v[236:239]\n"
        "v_mfma_f32_32x32x16_bf16 v[192:207], v[44:47], v[164:167], v[192:207]\n"
        "ds_read_b64_tr_b16 v[40:41], v11 offset:0\n"
        "ds_read_b64_tr_b16 v[42:43], v11 offset:512\n"
        "s_setprio 1\n"
        "s_add_u32 m0, s91, 0x15f80\n"
        "ds_read_b128 v[188:191], v227 offset:49152\n"
        "buffer_load_dwordx4 v1, s[40:43], s21 offen offset:128 lds\n"
        "s_waitcnt lgkmcnt(7)\n"
        "v_mfma_f32_32x32x16_bf16 v[192:207], v[28:31], v[168:171], v[192:207]\n"
        "ds_read_b64_tr_b16 v[44:45], v12 offset:0\n"
        "ds_read_b64_tr_b16 v[46:47], v12 offset:512\n"
        "v_mfma_f32_32x32x16_bf16 v[192:207], v[32:35], v[172:175], v[192:207]\n"
        "ds_read_b64_tr_b16 v[28:29], v13 offset:0\n"
        "ds_read_b64_tr_b16 v[30:31], v13 offset:512\n"
        "s_waitcnt lgkmcnt(5)\n"
        "v_mfma_f32_32x32x16_bf16 v[192:207], v[36:39], v[176:179], v[192:207]\n"
        "ds_read_b64_tr_b16 v[32:33], v6 offset:0\n"
        "ds_read_b64_tr_b16 v[34:35], v6 offset:512\n"
        "s_setprio 0\n"
        "s_add_u32 m0, s91, 0x17f00\n"
        "v_subrev_u32 v18, 10, v0\n"
        "v_cmp_gt_u32 vcc, 15, v18\n"
        "v_mov_b32 v18, 0x7ffffff0\n"
        "s_nop 0\n"
        "v_cndmask_b32 v19, v18, v14, vcc\n"
        "buffer_load_dword v237, v19, s[44:47], 0 offen offset:0\n"
        "v_mfma_f32_16x16x32_bf16 v[236:239], v[232:235], v[36:39], v[236:239]\n"
        "buffer_load_dwordx4 v1, s[40:43], s27 offen offset:256 lds\n"
        "v_mfma_f32_32x32x16_bf16 v[192:207], v[40:43], v[180:183], v[192:207]\n"
        "ds_read_b64_tr_b16 v[36:37], v7 offset:0\n"
        "ds_read_b64_tr_b16 v[38:39], v7 offset:512\n"
        "s_waitcnt lgkmcnt(4)\n"
        "v_mfma_f32_32x32x16_bf16 v[192:207], v[44:47], v[184:187], v[192:207]\n"
        "ds_read_b64_tr_b16 v[40:41], v8 offset:0\n"
        "ds_read_b64_tr_b16 v[42:43], v8 offset:512\n"
        "v_mfma_f32_32x32x16_bf16 v[192:207], v[28:31], v[188:191], v[192:207]\n"
        "ds_read_b64_tr_b16 v[44:45], v9 offset:0\n"
        "ds_read_b64_tr_b16 v[46:47], v9 offset:512\n"
        "v_subrev_u32 v18, 36, v0\n"
        "v_cmp_gt_u32 vcc, 9, v18\n"
        "v_mov_b32 v18, 0x7ffffff0\n"
        "s_nop 0\n"
        "v_cndmask_b32 v19, v18, v244, vcc\n"
        "buffer_load_dword v237, v19, s[40:43], 0 offen offset:0\n"
        "v_dot2c_f32_bf16 v248, v160, v242\n"
        "v_dot2c_f32_bf16 v250, v160, v160\n"
        "v_dot2c_f32_bf16 v249, v161, v242\n"
        "v_dot2c_f32_bf16 v251, v161, v161\n"
        "v_dot2c_f32_bf16 v248, v162, v242\n"
        "v_dot2c_f32_bf16 v250, v162, v162\n"
        "v_dot2c_f32_bf16 v249, v163, v242\n"
        "v_dot2c_f32_bf16 v251, v163, v163\n"
        "v_dot2c_f32_bf16 v248, v164, v242\n"
        "v_dot2c_f32_bf16 v250, v164, v164\n"
        "v_dot2c_f32_bf16 v249, v165, v242\n"
        "v_dot2c_f32_bf16 v251, v165, v165\n"
        "v_dot2c_f32_bf16 v248, v166, v242\n"
        "v_dot2c_f32_bf16 v250, v166, v166\n"
        "v_dot2c_f32_bf16 v249, v167, v242\n"
        "v_dot2c_f32_bf16 v251, v167, v167\n"
        "v_dot2c_f32_bf16 v248, v176, v242\n"
        "v_dot2c_f32_bf16 v250, v176, v176\n"
        "v_dot2c_f32_bf16 v249, v177, v242\n"
        "v_dot2c_f32_bf16 v251, v177, v177\n"
        "v_dot2c_f32_bf16 v248, v178, v242\n"
        "v_dot2c_f32_bf16 v250, v178, v178\n"
        "v_dot2c_f32_bf16 v249, v179, v242\n"
        "v_dot2c_f32_bf16 v251, v179, v179\n"
        "v_dot2c_f32_bf16 v248, v180, v242\n"
        "v_dot2c_f32_bf16 v250, v180, v180\n"
        "v_dot2c_f32_bf16 v249, v181, v242\n"
        "v_dot2c_f32_bf16 v251, v181, v181\n"
        "v_dot2c_f32_bf16 v248, v182, v242\n"
        "v_dot2c_f32_bf16 v250, v182, v182\n"
        "v_dot2c_f32_bf16 v249, v183, v242\n"
        "v_dot2c_f32_bf16 v251, v183, v183\n"
        "s_nop 1\n"
        "v_add_f32 v248, v248, v249\n"
        "v_add_f32 v249, v250, v251\n"
        "v_mov_b32 v250, v248\n"
        "v_mov_b32 v251, v249\n"
        "s_nop 0\n"
        "v_permlane32_swap_b32 v248, v250\n"
        "v_permlane32_swap_b32 v249, v251\n"
        "v_add_f32 v248, v248, v250\n"
        "v_add_f32 v249, v249, v251\n"
        "v_lshlrev_b32 v252, 3, v240\n"
        "s_lshl_b32 s92, %2, 8\n"
        "s_add_u32 s92, s92, 0x26400\n"
        "v_add_u32 v252, s92, v252\n"
        "ds_write_b64 v252, v[248:249] offset:0\n"
        "s_lshl_b32 s92, s22, 11\n"
        "v_subrev_u32 v252, s92, v252\n"
        "ds_write_b32 v24, v236 offset:0\n"
        "s_waitcnt lgkmcnt(0)\n"
        "s_waitcnt vmcnt(9)\n"
        "s_barrier\n"
        "ds_read2_b32 v[224:225], v25 offset0:0 offset1:32\n"
        "ds_read_b64 v[250:251], v252 offset:1024\n"
        "s_setprio 3\n"
        "s_add_u32 m0, s91, 0xbf00\n"
        "s_nop 0\n"
        "buffer_load_dwordx4 v1, s[40:43], s28 offen offset:256 lds\n"
        "v_mfma_f32_32x32x16_bf16 v[208:223], v[32:35], v[48:51], 0\n"
        "ds_read_b64_tr_b16 v[28:29], v6 offset:8192\n"
        "ds_read_b64_tr_b16 v[30:31], v6 offset:8704\n"
        "v_mfma_f32_16x16x32_bf16 v[236:239], v[232:235], v[32:35], 0\n"
        "v_mfma_f32_32x32x16_bf16 v[208:223], v[36:39], v[52:55], v[208:223]\n"
        "ds_read_b64_tr_b16 v[32:33], v7 offset:8192\n"
        "ds_read_b64_tr_b16 v[34:35], v7 offset:8704\n"
        "s_waitcnt lgkmcnt(4)\n"
        "v_add_f32 v248, v248, v250\n"
        "v_add_f32 v249, v249, v251\n"
        "v_mul_f32 v248, 0xbae38e39, v248\n"
        "v_mfma_f32_32x32x16_bf16 v[208:223], v[40:43], v[56:59], v[208:223]\n"
        "ds_read_b64_tr_b16 v[36:37], v8 offset:8192\n"
        "ds_read_b64_tr_b16 v[38:39], v8 offset:8704\n"
        "v_mul_f32 v249, 0x3ae38e39, v249\n"
        "v_fma_f32 v249, -v248, v248, v249\n"
        "v_max_f32 v249, 0, v249\n"
        "v_add_f32 v249, 0x3727c5ac, v249\n"
        "v_rsq_f32 v16, v249\n"
        "s_nop 0\n"
        "v_mul_f32 v20, v249, v16\n"
        "v_mov_b32 v21, v248\n"
        "v_mfma_f32_32x32x16_bf16 v[208:223], v[44:47], v[60:63], v[208:223]\n"
        "ds_read_b64_tr_b16 v[40:41], v9 offset:8192\n"
        "ds_read_b64_tr_b16 v[42:43], v9 offset:8704\n"
        "s_setprio 2\n"
        "s_add_u32 m0, s91, 0xdf00\n"
        "v_mul_f32 v17, v16, v16\n"
        "v_mul_f32 v17, v17, v16\n"
        "v_mul_f32 v17, 0xbdd2d3e8, v17\n"
        "v_mul_f32 v16, 0xc0135761, v16\n"
        "s_waitcnt vmcnt(6)\n"
        "v_add_f32 v224, v224, v225\n"
        "buffer_load_dwordx4 v1, s[40:43], s29 offen offset:256 lds\n"
        "s_waitcnt lgkmcnt(4)\n"
        "v_mfma_f32_32x32x16_bf16 v[208:223], v[28:31], v[64:67], v[208:223]\n"
        "ds_read_b64_tr_b16 v[44:45], v6 offset:16384\n"
        "ds_read_b64_tr_b16 v[46:47], v6 offset:16896\n"
        "v_lshlrev_b32 v26, 16, v26\n"
        "v_cvt_pk_bf16_f32 v229, v224, v26\n"
        "v_lshlrev_b32 v225, 16, v229\n"
        "v_sub_f32 v225, v224, v225\n"
        "v_cvt_pk_bf16_f32 v228, v224, v225\n"
        "v_cvt_pk_bf16_f32 v230, v26, 0\n"
        "v_mov_b32 v231, 0\n"
        "v_mfma_f32_16x16x32_bf16 v[236:239], v[232:235], v[28:31], v[236:239]\n"
        "v_mfma_f32_32x32x16_bf16 v[208:223], v[32:35], v[68:71], v[208:223]\n"
        "ds_read_b64_tr_b16 v[28:29], v7 offset:16384\n"
        "ds_read_b64_tr_b16 v[30:31], v7 offset:16896\n"
        "v_cvt_pk_bf16_f32 v224, v21, v21\n"
        "v_and_b32 v227, 0xffff0000, v224\n"
        "v_sub_f32 v227, v21, v227\n"
        "v_cvt_pk_bf16_f32 v225, v227, v20\n"
        "v_and_b32 v227, 0xffff0000, v225\n"
        "v_sub_f32 v227, v20, v227\n"
        "v_cvt_pk_bf16_f32 v226, v227, 0\n"
        "s_waitcnt lgkmcnt(4)\n"
        "v_mfma_f32_32x32x16_bf16 v[208:223], v[36:39], v[72:75], v[208:223]\n"
        "ds_read_b64_tr_b16 v[32:33], v8 offset:16384\n"
        "ds_read_b64_tr_b16 v[34:35], v8 offset:16896\n"
        "s_add_u32 m0, s91, 0xff00\n"
        "v_mov_b32 v227, 0\n"
        "s_nop 1\n"
        "v_mfma_f32_32x32x16_bf16 v[192:207], v[228:231], v[224:227], v[192:207]\n"
        "buffer_load_dwordx4 v1, s[40:43], s30 offen offset:256 lds\n"
        "v_mfma_f32_32x32x16_bf16 v[208:223], v[40:43], v[76:79], v[208:223]\n"
        "ds_read_b64_tr_b16 v[36:37], v9 offset:16384\n"
        "ds_read_b64_tr_b16 v[38:39], v9 offset:16896\n"
        "s_setprio 1\n"
        "s_waitcnt lgkmcnt(4)\n"
        "v_mfma_f32_32x32x16_bf16 v[208:223], v[44:47], v[80:83], v[208:223]\n"
        "ds_read_b64_tr_b16 v[40:41], v6 offset:24576\n"
        "ds_read_b64_tr_b16 v[42:43], v6 offset:25088\n"
        "s_nop 2\n"
        "v_mul_f32 v224, v192, v192\n"
        "v_mfma_f32_16x16x32_bf16 v[236:239], v[232:235], v[44:47], v[236:239]\n"
        "v_mfma_f32_32x32x16_bf16 v[208:223], v[28:31], v[84:87], v[208:223]\n"
        "ds_read_b64_tr_b16 v[44:45], v7 offset:24576\n"
        "ds_read_b64_tr_b16 v[46:47], v7 offset:25088\n"
        "s_add_u32 m0, s91, 0x19f00\n"
        "v_mul_f32 v225, v193, v193\n"
        "v_mul_f32 v226, v194, v194\n"
        "v_mul_f32 v227, v195, v195\n"
        "v_fma_f32 v224, v224, v17, v16\n"
        "v_mul_f32 v228, v196, v196\n"
        "v_fma_f32 v225, v225, v17, v16\n"
        "v_mul_f32 v229, v197, v197\n"
        "buffer_load_dwordx4 v1, s[40:43], s31 offen offset:256 lds\n"
        "s_waitcnt lgkmcnt(4)\n"
        "v_mfma_f32_32x32x16_bf16 v[208:223], v[32:35], v[88:91], v[208:223]\n"
        "ds_read_b64_tr_b16 v[28:29], v8 offset:24576\n"
        "ds_read_b64_tr_b16 v[30:31], v8 offset:25088\n"
        "v_fma_f32 v226, v226, v17, v16\n"
        "v_mul_f32 v230, v198, v198\n"
        "v_fma_f32 v227, v227, v17, v16\n"
        "v_mul_f32 v231, v199, v199\n"
        "v_mul_f32 v224, v224, v192\n"
        "v_fma_f32 v228, v228, v17, v16\n"
        "v_mfma_f32_32x32x16_bf16 v[208:223], v[36:39], v[92:95], v[208:223]\n"
        "ds_read_b64_tr_b16 v[32:33], v9 offset:24576\n"
        "ds_read_b64_tr_b16 v[34:35], v9 offset:25088\n"
        "s_setprio 0\n"
        "v_mul_f32 v225, v225, v193\n"
        "v_fma_f32 v229, v229, v17, v16\n"
        "v_mul_f32 v226, v226, v194\n"
        "v_fma_f32 v230, v230, v17, v16\n"
        "v_mul_f32 v227, v227, v195\n"
        "v_fma_f32 v231, v231, v17, v16\n"
        "v_subrev_u32 v18, 25, v0\n"
        "v_cmp_gt_u32 vcc, 15, v18\n"
        "v_mov_b32 v18, 0x7ffffff0\n"
        "s_nop 0\n"
        "v_cndmask_b32 v19, v18, v14, vcc\n"
        "buffer_load_dword v237, v19, s[44:47], 0 offen offset:0\n"
        "s_waitcnt lgkmcnt(4)\n"
        "v_mfma_f32_32x32x16_bf16 v[208:223], v[40:43], v[96:99], v[208:223]\n"
        "ds_read_b64_tr_b16 v[36:37], v10 offset:8192\n"
        "ds_read_b64_tr_b16 v[38:39], v10 offset:8704\n"
        "s_add_u32 m0, s91, 0x1bf00\n"
        "v_exp_f32 v224, v224\n"
        "v_mul_f32 v228, v228, v196\n"
        "v_exp_f32 v225, v225\n"
        "v_mul_f32 v229, v229, v197\n"
        "v_mfma_f32_16x16x32_bf16 v[236:239], v[232:235], v[40:43], v[236:239]\n"
        "buffer_load_dwordx4 v1, s[40:43], s68 offen offset:256 lds\n"
        "v_mfma_f32_32x32x16_bf16 v[208:223], v[44:47], v[100:103], v[208:223]\n"
        "ds_read_b64_tr_b16 v[40:41], v11 offset:8192\n"
        "ds_read_b64_tr_b16 v[42:43], v11 offset:8704\n"
        "v_exp_f32 v226, v226\n"
        "v_mul_f32 v230, v230, v198\n"
        "v_exp_f32 v227, v227\n"
        "v_mul_f32 v231, v231, v199\n"
        "s_waitcnt lgkmcnt(4)\n"
        "v_mfma_f32_32x32x16_bf16 v[208:223], v[28:31], v[104:107], v[208:223]\n"
        "ds_read_b64_tr_b16 v[44:45], v12 offset:8192\n"
        "ds_read_b64_tr_b16 v[46:47], v12 offset:8704\n"
        "v_fma_f32 v224, v224, v20, v20\n"
        "v_exp_f32 v228, v228\n"
        "v_fma_f32 v225, v225, v20, v20\n"
        "v_exp_f32 v229, v229\n"
        "v_mfma_f32_32x32x16_bf16 v[208:223], v[32:35], v[108:111], v[208:223]\n"
        "ds_read_b64_tr_b16 v[28:29], v13 offset:8192\n"
        "ds_read_b64_tr_b16 v[30:31], v13 offset:8704\n"
        "v_fma_f32 v226, v226, v20, v20\n"
        "v_exp_f32 v230, v230\n"
        "s_waitcnt vmcnt(7)\n"
        "s_barrier\n"
        "s_setprio 3\n"
        "v_fma_f32 v227, v227, v20, v20\n"
        "s_add_u32 m0, s91, 0x0\n"
        "s_add_u32 s92, s69, 0x100\n"
        "buffer_load_dwordx4 v1, s[40:43], s92 offen lds\n"
        "s_waitcnt lgkmcnt(4)\n"
        "v_mfma_f32_32x32x16_bf16 v[208:223], v[36:39], v[112:115], v[208:223]\n"
        "ds_read_b64_tr_b16 v[32:33], v10 offset:16384\n"
        "ds_read_b64_tr_b16 v[34:35], v10 offset:16896\n"
        "v_exp_f32 v231, v231\n"
        "v_rcp_f32 v224, v224\n"
        "v_fma_f32 v228, v228, v20, v20\n"
        "v_mfma_f32_16x16x32_bf16 v[236:239], v[232:235], v[36:39], v[236:239]\n"
        "v_mfma_f32_32x32x16_bf16 v[208:223], v[40:43], v[116:119], v[208:223]\n"
        "ds_read_b64_tr_b16 v[36:37], v11 offset:16384\n"
        "ds_read_b64_tr_b16 v[38:39], v11 offset:16896\n"
        "v_rcp_f32 v225, v225\n"
        "v_fma_f32 v229, v229, v20, v20\n"
        "v_rcp_f32 v226, v226\n"
        "v_lshlrev_b32 v26, 25, v0\n"
        "v_and_b32 v26, 0x40000000, v26\n"
        "v_lshl_add_u32 v26, v0, 1, v26\n"
        "s_lshl_b32 s93, s22, 6\n"
        "s_add_u32 s93, s93, s66\n"
        "buffer_load_ushort v26, v26, s[56:59], s93 offen offset:128\n"
        "s_waitcnt lgkmcnt(4)\n"
        "v_mfma_f32_32x32x16_bf16 v[208:223], v[44:47], v[120:123], v[208:223]\n"
        "ds_read_b64_tr_b16 v[40:41], v12 offset:16384\n"
        "ds_read_b64_tr_b16 v[42:43], v12 offset:16896\n"
        "v_fma_f32 v230, v230, v20, v20\n"
        "v_rcp_f32 v227, v227\n"
        "v_fma_f32 v231, v231, v20, v20\n"
        "v_mul_f32 v224, v224, v192\n"
        "v_rcp_f32 v228, v228\n"
        "v_mfma_f32_32x32x16_bf16 v[208:223], v[28:31], v[124:127], v[208:223]\n"
        "ds_read_b64_tr_b16 v[44:45], v13 offset:16384\n"
        "ds_read_b64_tr_b16 v[46:47], v13 offset:16896\n"
        "v_mul_f32 v225, v225, v193\n"
        "v_rcp_f32 v229, v229\n"
        "v_mul_f32 v226, v226, v194\n"
        "v_rcp_f32 v230, v230\n"
        "s_waitcnt lgkmcnt(4)\n"
        "v_mfma_f32_32x32x16_bf16 v[208:223], v[32:35], v[128:131], v[208:223]\n"
        "ds_read_b64_tr_b16 v[28:29], v6 offset:32768\n"
        "ds_read_b64_tr_b16 v[30:31], v6 offset:33280\n"
        "s_setprio 2\n"
        "s_add_u32 m0, s91, 0x1f00\n"
        "v_mul_f32 v227, v227, v195\n"
        "v_rcp_f32 v231, v231\n"
        "v_mul_f32 v228, v228, v196\n"
        "v_mul_f32 v229, v229, v197\n"
        "v_mfma_f32_16x16x32_bf16 v[236:239], v[232:235], v[32:35], v[236:239]\n"
        "buffer_load_dwordx4 v1, s[40:43], s20 offen offset:256 lds\n"
        "v_mfma_f32_32x32x16_bf16 v[208:223], v[36:39], v[132:135], v[208:223]\n"
        "ds_read_b64_tr_b16 v[32:33], v7 offset:32768\n"
        "ds_read_b64_tr_b16 v[34:35], v7 offset:33280\n"
        "v_mul_f32 v230, v230, v198\n"
        "v_mul_f32 v231, v231, v199\n"
        "v_cvt_pk_bf16_f32 v240, v224, v225\n"
        "v_cvt_pk_bf16_f32 v241, v226, v227\n"
        "v_cvt_pk_bf16_f32 v242, v228, v229\n"
        "v_cvt_pk_bf16_f32 v243, v230, v231\n"
        "s_waitcnt lgkmcnt(4)\n"
        "v_mfma_f32_32x32x16_bf16 v[208:223], v[40:43], v[136:139], v[208:223]\n"
        "ds_read_b64_tr_b16 v[36:37], v8 offset:32768\n"
        "ds_read_b64_tr_b16 v[38:39], v8 offset:33280\n"
        "v_mul_f32 v224, v200, v200\n"
        "v_mul_f32 v225, v201, v201\n"
        "v_mul_f32 v226, v202, v202\n"
        "v_mul_f32 v227, v203, v203\n"
        "v_fma_f32 v224, v224, v17, v16\n"
        "buffer_store_dwordx4 v[240:243], v5, s[48:51], s78 offen offset:0 sc1\n"
        "v_mfma_f32_32x32x16_bf16 v[208:223], v[44:47], v[140:143], v[208:223]\n"
        "ds_read_b64_tr_b16 v[40:41], v9 offset:32768\n"
        "ds_read_b64_tr_b16 v[42:43], v9 offset:33280\n"
        "v_mul_f32 v228, v204, v204\n"
        "v_fma_f32 v225, v225, v17, v16\n"
        "v_mul_f32 v229, v205, v205\n"
        "v_fma_f32 v226, v226, v17, v16\n"
        "v_mul_f32 v230, v206, v206\n"
        "v_fma_f32 v227, v227, v17, v16\n"
        "s_waitcnt lgkmcnt(4)\n"
        "v_mfma_f32_32x32x16_bf16 v[208:223], v[28:31], v[144:147], v[208:223]\n"
        "ds_read_b64_tr_b16 v[44:45], v6 offset:40960\n"
        "ds_read_b64_tr_b16 v[46:47], v6 offset:41472\n"
        "v_mul_f32 v231, v207, v207\n"
        "v_mul_f32 v224, v224, v200\n"
        "v_fma_f32 v228, v228, v17, v16\n"
        "v_mul_f32 v225, v225, v201\n"
        "v_fma_f32 v229, v229, v17, v16\n"
        "v_mul_f32 v226, v226, v202\n"
        "v_mfma_f32_16x16x32_bf16 v[236:239], v[232:235], v[28:31], v[236:239]\n"
        "v_mfma_f32_32x32x16_bf16 v[208:223], v[32:35], v[148:151], v[208:223]\n"
        "ds_read_b64_tr_b16 v[28:29], v7 offset:40960\n"
        "ds_read_b64_tr_b16 v[30:31], v7 offset:41472\n"
        "s_setprio 1\n"
        "s_add_u32 m0, s91, 0x3f00\n"
        "v_fma_f32 v230, v230, v17, v16\n"
        "v_mul_f32 v227, v227, v203\n"
        "v_fma_f32 v231, v231, v17, v16\n"
        "v_exp_f32 v224, v224\n"
        "v_mul_f32 v228, v228, v204\n"
        "buffer_load_dwordx4 v1, s[40:43], s21 offen offset:256 lds\n"
        "s_waitcnt lgkmcnt(4)\n"
        "v_mfma_f32_32x32x16_bf16 v[208:223], v[36:39], v[152:155], v[208:223]\n"
        "ds_read_b64_tr_b16 v[32:33], v8 offset:40960\n"
        "ds_read_b64_tr_b16 v[34:35], v8 offset:41472\n"
        "v_exp_f32 v225, v225\n"
        "v_mul_f32 v229, v229, v205\n"
        "v_exp_f32 v226, v226\n"
        "v_mul_f32 v230, v230, v206\n"
        "v_mfma_f32_32x32x16_bf16 v[208:223], v[40:43], v[156:159], v[208:223]\n"
        "ds_read_b64_tr_b16 v[36:37], v9 offset:40960\n"
        "ds_read_b64_tr_b16 v[38:39], v9 offset:41472\n"
        "v_exp_f32 v227, v227\n"
        "v_mul_f32 v231, v231, v207\n"
        "v_fma_f32 v224, v224, v20, v20\n"
        "v_exp_f32 v228, v228\n"
        "s_waitcnt lgkmcnt(4)\n"
        "v_mfma_f32_32x32x16_bf16 v[208:223], v[44:47], v[160:163], v[208:223]\n"
        "ds_read_b64_tr_b16 v[40:41], v10 offset:24576\n"
        "ds_read_b64_tr_b16 v[42:43], v10 offset:25088\n"
        "v_fma_f32 v225, v225, v20, v20\n"
        "v_exp_f32 v229, v229\n"
        "v_fma_f32 v226, v226, v20, v20\n"
        "v_exp_f32 v230, v230\n"
        "v_mfma_f32_16x16x32_bf16 v[236:239], v[232:235], v[44:47], v[236:239]\n"
        "v_mfma_f32_32x32x16_bf16 v[208:223], v[28:31], v[164:167], v[208:223]\n"
        "ds_read_b64_tr_b16 v[44:45], v11 offset:24576\n"
        "ds_read_b64_tr_b16 v[46:47], v11 offset:25088\n"
        "v_fma_f32 v227, v227, v20, v20\n"
        "v_exp_f32 v231, v231\n"
        "v_rcp_f32 v224, v224\n"
        "s_waitcnt lgkmcnt(4)\n"
        "v_mfma_f32_32x32x16_bf16 v[208:223], v[32:35], v[168:171], v[208:223]\n"
        "ds_read_b64_tr_b16 v[28:29], v12 offset:24576\n"
        "ds_read_b64_tr_b16 v[30:31], v12 offset:25088\n"
        "s_setprio 0\n"
        "s_add_u32 m0, s91, 0x5e80\n"
        "v_fma_f32 v228, v228, v20, v20\n"
        "v_rcp_f32 v225, v225\n"
        "v_fma_f32 v229, v229, v20, v20\n"
        "v_rcp_f32 v226, v226\n"
        "buffer_load_dwordx4 v1, s[40:43], s27 offen offset:384 lds\n"
        "v_mfma_f32_32x32x16_bf16 v[208:223], v[36:39], v[172:175], v[208:223]\n"
        "ds_read_b64_tr_b16 v[32:33], v13 offset:24576\n"
        "ds_read_b64_tr_b16 v[34:35], v13 offset:25088\n"
        "v_fma_f32 v230, v230, v20, v20\n"
        "v_rcp_f32 v227, v227\n"
        "v_fma_f32 v231, v231, v20, v20\n"
        "s_waitcnt lgkmcnt(4)\n"
        "v_mfma_f32_32x32x16_bf16 v[208:223], v[40:43], v[176:179], v[208:223]\n"
        "ds_read_b64_tr_b16 v[36:37], v10 offset:32768\n"
        "ds_read_b64_tr_b16 v[38:39], v10 offset:33280\n"
        "v_mul_f32 v224, v224, v200\n"
        "v_rcp_f32 v228, v228\n"
        "v_mul_f32 v225, v225, v201\n"
        "v_rcp_f32 v229, v229\n"
        "v_mfma_f32_16x16x32_bf16 v[236:239], v[232:235], v[40:43], v[236:239]\n"
        "v_mfma_f32_32x32x16_bf16 v[208:223], v[44:47], v[180:183], v[208:223]\n"
        "ds_read_b64_tr_b16 v[40:41], v11 offset:32768\n"
        "ds_read_b64_tr_b16 v[42:43], v11 offset:33280\n"
        "v_mul_f32 v226, v226, v202\n"
        "v_rcp_f32 v230, v230\n"
        "v_mul_f32 v227, v227, v203\n"
        "v_rcp_f32 v231, v231\n"
        "s_waitcnt lgkmcnt(4)\n"
        "v_mfma_f32_32x32x16_bf16 v[208:223], v[28:31], v[184:187], v[208:223]\n"
        "ds_read_b64_tr_b16 v[44:45], v12 offset:32768\n"
        "ds_read_b64_tr_b16 v[46:47], v12 offset:33280\n"
        "v_mul_f32 v228, v228, v204\n"
        "v_mul_f32 v229, v229, v205\n"
        "v_mul_f32 v230, v230, v206\n"
        "v_mul_f32 v231, v231, v207\n"
        "v_cvt_pk_bf16_f32 v244, v224, v225\n"
        "v_cvt_pk_bf16_f32 v245, v226, v227\n"
        "v_mfma_f32_32x32x16_bf16 v[208:223], v[32:35], v[188:191], v[208:223]\n"
        "ds_read_b64_tr_b16 v[28:29], v13 offset:32768\n"
        "ds_read_b64_tr_b16 v[30:31], v13 offset:33280\n"
        "v_cvt_pk_bf16_f32 v246, v228, v229\n"
        "v_cvt_pk_bf16_f32 v247, v230, v231\n"
        "buffer_store_dwordx4 v[244:247], v5, s[48:51], s78 offen offset:1024 sc1\n"
        "ds_write_b32 v24, v236 offset:0\n"
        "s_waitcnt lgkmcnt(0)\n"
        "s_waitcnt vmcnt(9)\n"
        "s_barrier\n"
        "ds_read2_b32 v[224:225], v25 offset0:0 offset1:32\n"
        "s_setprio 3\n"
        "s_add_u32 m0, s91, 0x7e80\n"
        "s_nop 0\n"
        "buffer_load_dwordx4 v1, s[40:43], s28 offen offset:384 lds\n"
        "v_mfma_f32_32x32x16_bf16 v[192:207], v[36:39], v[48:51], 0\n"
        "ds_read_b64_tr_b16 v[32:33], v6 offset:49152\n"
        "ds_read_b64_tr_b16 v[34:35], v6 offset:49664\n"
        "v_mfma_f32_16x16x32_bf16 v[236:239], v[232:235], v[36:39], 0\n"
        "v_mfma_f32_32x32x16_bf16 v[192:207], v[40:43], v[52:55], v[192:207]\n"
        "ds_read_b64_tr_b16 v[36:37], v7 offset:49152\n"
        "ds_read_b64_tr_b16 v[38:39], v7 offset:49664\n"
        "s_waitcnt lgkmcnt(4)\n"
        "s_waitcnt vmcnt(6)\n"
        "v_add_f32 v224, v224, v225\n"
        "v_lshlrev_b32 v26, 16, v26\n"
        "v_cvt_pk_bf16_f32 v229, v224, v26\n"
        "v_mfma_f32_32x32x16_bf16 v[192:207], v[44:47], v[56:59], v[192:207]\n"
        "ds_read_b64_tr_b16 v[40:41], v8 offset:49152\n"
        "ds_read_b64_tr_b16 v[42:43], v8 offset:49664\n"
        "v_lshlrev_b32 v225, 16, v229\n"
        "v_sub_f32 v225, v224, v225\n"
        "v_cvt_pk_bf16_f32 v228, v224, v225\n"
        "v_cvt_pk_bf16_f32 v230, v26, 0\n"
        "v_mov_b32 v231, 0\n"
        "v_cvt_pk_bf16_f32 v224, v21, v21\n"
        "v_and_b32 v227, 0xffff0000, v224\n"
        "v_mfma_f32_32x32x16_bf16 v[192:207], v[28:31], v[60:63], v[192:207]\n"
        "ds_read_b64_tr_b16 v[44:45], v9 offset:49152\n"
        "ds_read_b64_tr_b16 v[46:47], v9 offset:49664\n"
        "s_setprio 2\n"
        "s_add_u32 m0, s91, 0x9e80\n"
        "v_sub_f32 v227, v21, v227\n"
        "v_cvt_pk_bf16_f32 v225, v227, v20\n"
        "v_and_b32 v227, 0xffff0000, v225\n"
        "v_sub_f32 v227, v20, v227\n"
        "v_cvt_pk_bf16_f32 v226, v227, 0\n"
        "v_mov_b32 v227, 0\n"
        "s_nop 1\n"
        "v_mfma_f32_32x32x16_bf16 v[208:223], v[228:231], v[224:227], v[208:223]\n"
        "buffer_load_dwordx4 v1, s[40:43], s29 offen offset:384 lds\n"
        "s_waitcnt lgkmcnt(4)\n"
        "v_mfma_f32_32x32x16_bf16 v[192:207], v[32:35], v[64:67], v[192:207]\n"
        "ds_read_b64_tr_b16 v[28:29], v6 offset:57344\n"
        "ds_read_b64_tr_b16 v[30:31], v6 offset:57856\n"
        "v_mfma_f32_16x16x32_bf16 v[236:239], v[232:235], v[32:35], v[236:239]\n"
        "v_mfma_f32_32x32x16_bf16 v[192:207], v[36:39], v[68:71], v[192:207]\n"
        "ds_read_b64_tr_b16 v[32:33], v7 offset:57344\n"
        "ds_read_b64_tr_b16 v[34:35], v7 offset:57856\n"
        "s_waitcnt lgkmcnt(4)\n"
        "v_mfma_f32_32x32x16_bf16 v[192:207], v[40:43], v[72:75], v[192:207]\n"
        "ds_read_b64_tr_b16 v[36:37], v8 offset:57344\n"
        "ds_read_b64_tr_b16 v[38:39], v8 offset:57856\n"
        "s_add_u32 m0, s91, 0x11e80\n"
        "v_mul_f32 v224, v208, v208\n"
        "v_mul_f32 v225, v209, v209\n"
        "v_mul_f32 v226, v210, v210\n"
        "v_mul_f32 v227, v211, v211\n"
        "buffer_load_dwordx4 v1, s[40:43], s30 offen offset:384 lds\n"
        "v_mfma_f32_32x32x16_bf16 v[192:207], v[44:47], v[76:79], v[192:207]\n"
        "ds_read_b64_tr_b16 v[40:41], v9 offset:57344\n"
        "ds_read_b64_tr_b16 v[42:43], v9 offset:57856\n"
        "s_setprio 1\n"
        "v_fma_f32 v224, v224, v17, v16\n"
        "v_mul_f32 v228, v212, v212\n"
        "v_fma_f32 v225, v225, v17, v16\n"
        "v_mul_f32 v229, v213, v213\n"
        "v_fma_f32 v226, v226, v17, v16\n"
        "s_waitcnt vmcnt(3)\n"
        "v_mov_b32 v18, 1\n"
        "v_mov_b32 v19, s65\n"
        "buffer_store_dwordx2 v[18:19], v22, s[48:51], 0 offen offset:0 sc1\n"
        "s_waitcnt lgkmcnt(4)\n"
        "v_mfma_f32_32x32x16_bf16 v[192:207], v[28:31], v[80:83], v[192:207]\n"
        "ds_read_b64_tr_b16 v[44:45], v10 offset:0\n"
        "ds_read_b64_tr_b16 v[46:47], v10 offset:512\n"
        "v_mul_f32 v230, v214, v214\n"
        "v_fma_f32 v227, v227, v17, v16\n"
        "v_mul_f32 v231, v215, v215\n"
        "v_mul_f32 v224, v224, v208\n"
        "v_fma_f32 v228, v228, v17, v16\n"
        "v_mul_f32 v225, v225, v209\n"
        "v_mfma_f32_16x16x32_bf16 v[236:239], v[232:235], v[28:31], v[236:239]\n"
        "v_mfma_f32_32x32x16_bf16 v[192:207], v[32:35], v[84:87], v[192:207]\n"
        "ds_read_b64_tr_b16 v[28:29], v11 offset:0\n"
        "ds_read_b64_tr_b16 v[30:31], v11 offset:512\n"
        "s_add_u32 m0, s91, 0x13e80\n"
        "v_fma_f32 v229, v229, v17, v16\n"
        "v_mul_f32 v226, v226, v210\n"
        "v_fma_f32 v230, v230, v17, v16\n"
        "v_mul_f32 v227, v227, v211\n"
        "v_fma_f32 v231, v231, v17, v16\n"
        "v_exp_f32 v224, v224\n"
        "buffer_load_dwordx4 v1, s[40:43], s31 offen offset:384 lds\n"
        "s_waitcnt lgkmcnt(4)\n"
        "v_mfma_f32_32x32x16_bf16 v[192:207], v[36:39], v[88:91], v[192:207]\n"
        "ds_read_b64_tr_b16 v[32:33], v12 offset:0\n"
        "ds_read_b64_tr_b16 v[34:35], v12 offset:512\n"
        "v_mul_f32 v228, v228, v212\n"
        "v_exp_f32 v225, v225\n"
        "v_mul_f32 v229, v229, v213\n"
        "v_mfma_f32_32x32x16_bf16 v[192:207], v[40:43], v[92:95], v[192:207]\n"
        "ds_read_b64_tr_b16 v[36:37], v13 offset:0\n"
        "ds_read_b64_tr_b16 v[38:39], v13 offset:512\n"
        "s_setprio 0\n"
        "v_exp_f32 v226, v226\n"
        "v_mul_f32 v230, v230, v214\n"
        "v_exp_f32 v227, v227\n"
        "v_mul_f32 v231, v231, v215\n"
        "s_waitcnt lgkmcnt(4)\n"
        "v_mfma_f32_32x32x16_bf16 v[192:207], v[44:47], v[96:99], v[192:207]\n"
        "ds_read_b64_tr_b16 v[40:41], v10 offset:40960\n"
        "ds_read_b64_tr_b16 v[42:43], v10 offset:41472\n"
        "s_add_u32 m0, s91, 0x15e80\n"
        "v_fma_f32 v224, v224, v20, v20\n"
        "v_exp_f32 v228, v228\n"
        "v_fma_f32 v225, v225, v20, v20\n"
        "v_exp_f32 v229, v229\n"
        "v_mfma_f32_16x16x32_bf16 v[236:239], v[232:235], v[44:47], v[236:239]\n"
        "buffer_load_dwordx4 v1, s[40:43], s68 offen offset:384 lds\n"
        "v_mfma_f32_32x32x16_bf16 v[192:207], v[28:31], v[100:103], v[192:207]\n"
        "ds_read_b64_tr_b16 v[44:45], v11 offset:40960\n"
        "ds_read_b64_tr_b16 v[46:47], v11 offset:41472\n"
        "v_fma_f32 v226, v226, v20, v20\n"
        "v_exp_f32 v230, v230\n"
        "v_fma_f32 v227, v227, v20, v20\n"
        "s_waitcnt lgkmcnt(4)\n"
        "v_mfma_f32_32x32x16_bf16 v[192:207], v[32:35], v[104:107], v[192:207]\n"
        "ds_read_b64_tr_b16 v[28:29], v12 offset:40960\n"
        "ds_read_b64_tr_b16 v[30:31], v12 offset:41472\n"
        "v_exp_f32 v231, v231\n"
        "v_rcp_f32 v224, v224\n"
        "v_fma_f32 v228, v228, v20, v20\n"
        "v_mfma_f32_32x32x16_bf16 v[192:207], v[36:39], v[108:111], v[192:207]\n"
        "ds_read_b64_tr_b16 v[32:33], v13 offset:40960\n"
        "ds_read_b64_tr_b16 v[34:35], v13 offset:41472\n"
        "v_rcp_f32 v225, v225\n"
        "v_fma_f32 v229, v229, v20, v20\n"
        "s_barrier\n"
        "s_setprio 3\n"
        "s_add_u32 m0, s91, 0xbe80\n"
        "v_rcp_f32 v226, v226\n"
        "buffer_load_dwordx4 v1, s[40:43], s69 offen offset:384 lds\n"
        "s_waitcnt lgkmcnt(4)\n"
        "v_mfma_f32_32x32x16_bf16 v[192:207], v[40:43], v[112:115], v[192:207]\n"
        "ds_read_b64_tr_b16 v[36:37], v10 offset:49152\n"
        "ds_read_b64_tr_b16 v[38:39], v10 offset:49664\n"
        "v_fma_f32 v230, v230, v20, v20\n"
        "v_rcp_f32 v227, v227\n"
        "v_mfma_f32_16x16x32_bf16 v[236:239], v[232:235], v[40:43], v[236:239]\n"
        "v_mfma_f32_32x32x16_bf16 v[192:207], v[44:47], v[116:119], v[192:207]\n"
        "ds_read_b64_tr_b16 v[40:41], v11 offset:49152\n"
        "ds_read_b64_tr_b16 v[42:43], v11 offset:49664\n"
        "v_fma_f32 v231, v231, v20, v20\n"
        "v_mul_f32 v224, v224, v208\n"
        "v_rcp_f32 v228, v228\n"
        "v_mul_f32 v225, v225, v209\n"
        "v_rcp_f32 v229, v229\n"
        "v_lshlrev_b32 v26, 25, v0\n"
        "v_and_b32 v26, 0x40000000, v26\n"
        "v_lshl_add_u32 v26, v0, 1, v26\n"
        "s_lshl_b32 s93, s22, 6\n"
        "s_add_u32 s93, s93, s66\n"
        "buffer_load_ushort v26, v26, s[56:59], s93 offen offset:256\n"
        "s_waitcnt lgkmcnt(4)\n"
        "v_mfma_f32_32x32x16_bf16 v[192:207], v[28:31], v[120:123], v[192:207]\n"
        "ds_read_b64_tr_b16 v[44:45], v12 offset:49152\n"
        "ds_read_b64_tr_b16 v[46:47], v12 offset:49664\n"
        "v_mul_f32 v226, v226, v210\n"
        "v_rcp_f32 v230, v230\n"
        "v_mul_f32 v227, v227, v211\n"
        "v_mfma_f32_32x32x16_bf16 v[192:207], v[32:35], v[124:127], v[192:207]\n"
        "ds_read_b64_tr_b16 v[28:29], v13 offset:49152\n"
        "ds_read_b64_tr_b16 v[30:31], v13 offset:49664\n"
        "v_rcp_f32 v231, v231\n"
        "v_mul_f32 v228, v228, v212\n"
        "v_mul_f32 v229, v229, v213\n"
        "v_mul_f32 v230, v230, v214\n"
        "v_mul_f32 v231, v231, v215\n"
        "s_waitcnt lgkmcnt(4)\n"
        "v_mfma_f32_32x32x16_bf16 v[192:207], v[36:39], v[128:131], v[192:207]\n"
        "ds_read_b64_tr_b16 v[32:33], v6 offset:0\n"
        "ds_read_b64_tr_b16 v[34:35], v6 offset:512\n"
        "s_setprio 2\n"
        "s_add_u32 m0, s91, 0xde80\n"
        "v_cvt_pk_bf16_f32 v248, v224, v225\n"
        "v_cvt_pk_bf16_f32 v249, v226, v227\n"
        "v_cvt_pk_bf16_f32 v250, v228, v229\n"
        "v_cvt_pk_bf16_f32 v251, v230, v231\n"
        "v_mfma_f32_16x16x32_bf16 v[236:239], v[232:235], v[36:39], v[236:239]\n"
        "buffer_load_dwordx4 v1, s[40:43], s20 offen offset:384 lds\n"
        "buffer_store_dwordx4 v[248:251], v5, s[48:51], s78 offen offset:2048 sc1\n"
        "v_mfma_f32_32x32x16_bf16 v[192:207], v[40:43], v[132:135], v[192:207]\n"
        "ds_read_b64_tr_b16 v[36:37], v7 offset:0\n"
        "ds_read_b64_tr_b16 v[38:39], v7 offset:512\n"
        "v_mul_f32 v224, v216, v216\n"
        "v_mul_f32 v225, v217, v217\n"
        "v_mul_f32 v226, v218, v218\n"
        "v_mul_f32 v227, v219, v219\n"
        "v_fma_f32 v224, v224, v17, v16\n"
        "v_mul_f32 v228, v220, v220\n"
        "s_waitcnt lgkmcnt(4)\n"
        "v_mfma_f32_32x32x16_bf16 v[192:207], v[44:47], v[136:139], v[192:207]\n"
        "ds_read_b64_tr_b16 v[40:41], v8 offset:0\n"
        "ds_read_b64_tr_b16 v[42:43], v8 offset:512\n"
        "v_fma_f32 v225, v225, v17, v16\n"
        "v_mul_f32 v229, v221, v221\n"
        "v_fma_f32 v226, v226, v17, v16\n"
        "v_mul_f32 v230, v222, v222\n"
        "v_fma_f32 v227, v227, v17, v16\n"
        "v_mfma_f32_32x32x16_bf16 v[192:207], v[28:31], v[140:143], v[192:207]\n"
        "ds_read_b64_tr_b16 v[44:45], v9 offset:0\n"
        "ds_read_b64_tr_b16 v[46:47], v9 offset:512\n"
        "v_mul_f32 v231, v223, v223\n"
        "v_mul_f32 v224, v224, v216\n"
        "v_fma_f32 v228, v228, v17, v16\n"
        "v_mul_f32 v225, v225, v217\n"
        "v_fma_f32 v229, v229, v17, v16\n"
        "v_mul_f32 v226, v226, v218\n"
        "s_waitcnt lgkmcnt(4)\n"
        "v_mfma_f32_32x32x16_bf16 v[192:207], v[32:35], v[144:147], v[192:207]\n"
        "ds_read_b64_tr_b16 v[28:29], v6 offset:8192\n"
        "ds_read_b64_tr_b16 v[30:31], v6 offset:8704\n"
        "v_fma_f32 v230, v230, v17, v16\n"
        "v_mul_f32 v227, v227, v219\n"
        "v_fma_f32 v231, v231, v17, v16\n"
        "v_exp_f32 v224, v224\n"
        "v_mfma_f32_16x16x32_bf16 v[236:239], v[232:235], v[32:35], v[236:239]\n"
        "v_mfma_f32_32x32x16_bf16 v[192:207], v[36:39], v[148:151], v[192:207]\n"
        "ds_read_b64_tr_b16 v[32:33], v7 offset:8192\n"
        "ds_read_b64_tr_b16 v[34:35], v7 offset:8704\n"
        "s_setprio 1\n"
        "s_add_u32 m0, s91, 0xfe80\n"
        "v_mul_f32 v228, v228, v220\n"
        "v_exp_f32 v225, v225\n"
        "v_mul_f32 v229, v229, v221\n"
        "v_exp_f32 v226, v226\n"
        "buffer_load_dwordx4 v1, s[40:43], s21 offen offset:384 lds\n"
        "s_waitcnt lgkmcnt(4)\n"
        "v_mfma_f32_32x32x16_bf16 v[192:207], v[40:43], v[152:155], v[192:207]\n"
        "ds_read_b64_tr_b16 v[36:37], v8 offset:8192\n"
        "ds_read_b64_tr_b16 v[38:39], v8 offset:8704\n"
        "v_mul_f32 v230, v230, v222\n"
        "v_exp_f32 v227, v227\n"
        "v_mul_f32 v231, v231, v223\n"
        "v_fma_f32 v224, v224, v20, v20\n"
        "v_mfma_f32_32x32x16_bf16 v[192:207], v[44:47], v[156:159], v[192:207]\n"
        "ds_read_b64_tr_b16 v[40:41], v9 offset:8192\n"
        "ds_read_b64_tr_b16 v[42:43], v9 offset:8704\n"
        "v_exp_f32 v228, v228\n"
        "v_fma_f32 v225, v225, v20, v20\n"
        "v_exp_f32 v229, v229\n"
        "s_waitcnt lgkmcnt(4)\n"
        "v_mfma_f32_32x32x16_bf16 v[192:207], v[28:31], v[160:163], v[192:207]\n"
        "ds_read_b64_tr_b16 v[44:45], v6 offset:16384\n"
        "ds_read_b64_tr_b16 v[46:47], v6 offset:16896\n"
        "v_fma_f32 v226, v226, v20, v20\n"
        "v_exp_f32 v230, v230\n"
        "v_fma_f32 v227, v227, v20, v20\n"
        "v_exp_f32 v231, v231\n"
        "v_mfma_f32_16x16x32_bf16 v[236:239], v[232:235], v[28:31], v[236:239]\n"
        "v_mfma_f32_32x32x16_bf16 v[192:207], v[32:35], v[164:167], v[192:207]\n"
        "ds_read_b64_tr_b16 v[28:29], v7 offset:16384\n"
        "ds_read_b64_tr_b16 v[30:31], v7 offset:16896\n"
        "v_rcp_f32 v224, v224\n"
        "v_fma_f32 v228, v228, v20, v20\n"
        "v_rcp_f32 v225, v225\n"
        "s_waitcnt lgkmcnt(4)\n"
        "v_mfma_f32_32x32x16_bf16 v[192:207], v[36:39], v[168:171], v[192:207]\n"
        "ds_read_b64_tr_b16 v[32:33], v8 offset:16384\n"
        "ds_read_b64_tr_b16 v[34:35], v8 offset:16896\n"
        "s_setprio 0\n"
        "v_fma_f32 v229, v229, v20, v20\n"
        "v_rcp_f32 v226, v226\n"
        "v_fma_f32 v230, v230, v20, v20\n"
        "s_add_u32 m0, s91, 0x18000\n"
        "s_cmp_eq_u32 s22, 0\n"
        "s_cselect_b32 s92, s27, s28\n"
        "s_add_u32 s92, s92, s88\n"
        "buffer_load_dwordx4 v2, s[40:43], s92 offen lds\n"
        "v_mfma_f32_32x32x16_bf16 v[192:207], v[40:43], v[172:175], v[192:207]\n"
        "ds_read_b64_tr_b16 v[36:37], v9 offset:16384\n"
        "ds_read_b64_tr_b16 v[38:39], v9 offset:16896\n"
        "v_rcp_f32 v227, v227\n"
        "v_fma_f32 v231, v231, v20, v20\n"
        "v_mul_f32 v224, v224, v216\n"
        "v_rcp_f32 v228, v228\n"
        "s_waitcnt lgkmcnt(4)\n"
        "v_mfma_f32_32x32x16_bf16 v[192:207], v[44:47], v[176:179], v[192:207]\n"
        "ds_read_b64_tr_b16 v[40:41], v6 offset:24576\n"
        "ds_read_b64_tr_b16 v[42:43], v6 offset:25088\n"
        "v_mul_f32 v225, v225, v217\n"
        "v_rcp_f32 v229, v229\n"
        "v_mul_f32 v226, v226, v218\n"
        "v_mfma_f32_16x16x32_bf16 v[236:239], v[232:235], v[44:47], v[236:239]\n"
        "v_mfma_f32_32x32x16_bf16 v[192:207], v[28:31], v[180:183], v[192:207]\n"
        "ds_read_b64_tr_b16 v[44:45], v7 offset:24576\n"
        "ds_read_b64_tr_b16 v[46:47], v7 offset:25088\n"
        "v_rcp_f32 v230, v230\n"
        "v_mul_f32 v227, v227, v219\n"
        "v_rcp_f32 v231, v231\n"
        "v_mul_f32 v228, v228, v220\n"
        "s_waitcnt lgkmcnt(4)\n"
        "v_mfma_f32_32x32x16_bf16 v[192:207], v[32:35], v[184:187], v[192:207]\n"
        "ds_read_b64_tr_b16 v[28:29], v8 offset:24576\n"
        "ds_read_b64_tr_b16 v[30:31], v8 offset:25088\n"
        "v_mul_f32 v229, v229, v221\n"
        "v_mul_f32 v230, v230, v222\n"
        "v_mul_f32 v231, v231, v223\n"
        "v_cvt_pk_bf16_f32 v252, v224, v225\n"
        "v_cvt_pk_bf16_f32 v253, v226, v227\n"
        "v_mfma_f32_32x32x16_bf16 v[192:207], v[36:39], v[188:191], v[192:207]\n"
        "ds_read_b64_tr_b16 v[32:33], v9 offset:24576\n"
        "ds_read_b64_tr_b16 v[34:35], v9 offset:25088\n"
        "v_cvt_pk_bf16_f32 v254, v228, v229\n"
        "v_cvt_pk_bf16_f32 v255, v230, v231\n"
        "buffer_store_dwordx4 v[252:255], v5, s[48:51], s78 offen offset:3072 sc1\n"
        "ds_write_b32 v24, v236 offset:0\n"
        "s_waitcnt lgkmcnt(0)\n"
        "s_waitcnt vmcnt(8)\n"
        "s_barrier\n"
        "ds_read2_b32 v[224:225], v25 offset0:0 offset1:32\n"
        "s_setprio 3\n"
        "s_add_u32 m0, s91, 0x0\n"
        "s_cmp_eq_u32 s22, 0\n"
        "s_cselect_b32 s92, s29, s30\n"
        "s_add_u32 s92, s92, s88\n"
        "buffer_load_dwordx4 v2, s[40:43], s92 offen lds\n"
        "v_mfma_f32_32x32x16_bf16 v[208:223], v[40:43], v[48:51], 0\n"
        "ds_read_b64_tr_b16 v[36:37], v6 offset:32768\n"
        "ds_read_b64_tr_b16 v[38:39], v6 offset:33280\n"
        "v_mfma_f32_16x16x32_bf16 v[236:239], v[232:235], v[40:43], 0\n"
        "v_mfma_f32_32x32x16_bf16 v[208:223], v[44:47], v[52:55], v[208:223]\n"
        "ds_read_b64_tr_b16 v[40:41], v7 offset:32768\n"
        "ds_read_b64_tr_b16 v[42:43], v7 offset:33280\n"
        "s_waitcnt lgkmcnt(4)\n"
        "s_waitcnt vmcnt(6)\n"
        "v_add_f32 v224, v224, v225\n"
        "v_lshlrev_b32 v26, 16, v26\n"
        "v_cvt_pk_bf16_f32 v229, v224, v26\n"
        "v_mfma_f32_32x32x16_bf16 v[208:223], v[28:31], v[56:59], v[208:223]\n"
        "ds_read_b64_tr_b16 v[44:45], v8 offset:32768\n"
        "ds_read_b64_tr_b16 v[46:47], v8 offset:33280\n"
        "v_lshlrev_b32 v225, 16, v229\n"
        "v_sub_f32 v225, v224, v225\n"
        "v_cvt_pk_bf16_f32 v228, v224, v225\n"
        "v_cvt_pk_bf16_f32 v230, v26, 0\n"
        "v_mov_b32 v231, 0\n"
        "v_cvt_pk_bf16_f32 v224, v21, v21\n"
        "v_and_b32 v227, 0xffff0000, v224\n"
        "v_mfma_f32_32x32x16_bf16 v[208:223], v[32:35], v[60:63], v[208:223]\n"
        "ds_read_b64_tr_b16 v[28:29], v9 offset:32768\n"
        "ds_read_b64_tr_b16 v[30:31], v9 offset:33280\n"
        "s_setprio 2\n"
        "v_sub_f32 v227, v21, v227\n"
        "v_cvt_pk_bf16_f32 v225, v227, v20\n"
        "v_and_b32 v227, 0xffff0000, v225\n"
        "v_sub_f32 v227, v20, v227\n"
        "v_cvt_pk_bf16_f32 v226, v227, 0\n"
        "v_mov_b32 v227, 0\n"
        "s_nop 1\n"
        "v_mfma_f32_32x32x16_bf16 v[192:207], v[228:231], v[224:227], v[192:207]\n"
        "s_add_u32 m0, s91, 0x2000\n"
        "s_cmp_eq_u32 s22, 0\n"
        "s_cselect_b32 s92, s31, s68\n"
        "s_add_u32 s92, s92, s88\n"
        "buffer_load_dwordx4 v2, s[40:43], s92 offen lds\n"
        "s_waitcnt lgkmcnt(4)\n"
        "v_mfma_f32_32x32x16_bf16 v[208:223], v[36:39], v[64:67], v[208:223]\n"
        "ds_read_b64_tr_b16 v[32:33], v6 offset:40960\n"
        "ds_read_b64_tr_b16 v[34:35], v6 offset:41472\n"
        "v_mfma_f32_16x16x32_bf16 v[236:239], v[232:235], v[36:39], v[236:239]\n"
        "v_mfma_f32_32x32x16_bf16 v[208:223], v[40:43], v[68:71], v[208:223]\n"
        "ds_read_b64_tr_b16 v[36:37], v7 offset:40960\n"
        "ds_read_b64_tr_b16 v[38:39], v7 offset:41472\n"
        "s_waitcnt lgkmcnt(4)\n"
        "v_mfma_f32_32x32x16_bf16 v[208:223], v[44:47], v[72:75], v[208:223]\n"
        "ds_read_b64_tr_b16 v[40:41], v8 offset:40960\n"
        "ds_read_b64_tr_b16 v[42:43], v8 offset:41472\n"
        "v_mul_f32 v224, v192, v192\n"
        "v_mul_f32 v225, v193, v193\n"
        "v_mul_f32 v226, v194, v194\n"
        "v_mul_f32 v227, v195, v195\n"
        "s_add_u32 m0, s91, 0x4000\n"
        "s_cmp_eq_u32 s22, 0\n"
        "s_cselect_b32 s92, s69, s20\n"
        "s_add_u32 s92, s92, s88\n"
        "buffer_load_dwordx4 v2, s[40:43], s92 offen lds\n"
        "v_mfma_f32_32x32x16_bf16 v[208:223], v[28:31], v[76:79], v[208:223]\n"
        "ds_read_b64_tr_b16 v[44:45], v9 offset:40960\n"
        "ds_read_b64_tr_b16 v[46:47], v9 offset:41472\n"
        "s_setprio 1\n"
        "v_fma_f32 v224, v224, v17, v16\n"
        "v_mul_f32 v228, v196, v196\n"
        "v_fma_f32 v225, v225, v17, v16\n"
        "v_mul_f32 v229, v197, v197\n"
        "v_fma_f32 v226, v226, v17, v16\n"
        "s_waitcnt vmcnt(3)\n"
        "v_mov_b32 v18, 2\n"
        "v_mov_b32 v19, s65\n"
        "buffer_store_dwordx2 v[18:19], v22, s[48:51], 0 offen offset:0 sc1\n"
        "s_waitcnt lgkmcnt(4)\n"
        "v_mfma_f32_32x32x16_bf16 v[208:223], v[32:35], v[80:83], v[208:223]\n"
        "ds_read_b64_tr_b16 v[28:29], v10 offset:8192\n"
        "ds_read_b64_tr_b16 v[30:31], v10 offset:8704\n"
        "v_mul_f32 v230, v198, v198\n"
        "v_fma_f32 v227, v227, v17, v16\n"
        "v_mul_f32 v231, v199, v199\n"
        "v_mul_f32 v224, v224, v192\n"
        "v_fma_f32 v228, v228, v17, v16\n"
        "v_mul_f32 v225, v225, v193\n"
        "v_mfma_f32_16x16x32_bf16 v[236:239], v[232:235], v[32:35], v[236:239]\n"
        "v_mfma_f32_32x32x16_bf16 v[208:223], v[36:39], v[84:87], v[208:223]\n"
        "ds_read_b64_tr_b16 v[32:33], v11 offset:8192\n"
        "ds_read_b64_tr_b16 v[34:35], v11 offset:8704\n"
        "v_fma_f32 v229, v229, v17, v16\n"
        "v_mul_f32 v226, v226, v194\n"
        "v_fma_f32 v230, v230, v17, v16\n"
        "v_mul_f32 v227, v227, v195\n"
        "v_fma_f32 v231, v231, v17, v16\n"
        "v_exp_f32 v224, v224\n"
        "s_add_u32 m0, s91, 0x1a000\n"
        "s_cmp_eq_u32 s22, 0\n"
        "s_cselect_b32 s92, s21, s21\n"
        "s_add_u32 s92, s92, s88\n"
        "buffer_load_dwordx4 v2, s[40:43], s92 offen lds\n"
        "s_waitcnt lgkmcnt(4)\n"
        "v_mfma_f32_32x32x16_bf16 v[208:223], v[40:43], v[88:91], v[208:223]\n"
        "ds_read_b64_tr_b16 v[36:37], v12 offset:8192\n"
        "ds_read_b64_tr_b16 v[38:39], v12 offset:8704\n"
        "v_mul_f32 v228, v228, v196\n"
        "v_exp_f32 v225, v225\n"
        "v_mul_f32 v229, v229, v197\n"
        "v_mfma_f32_32x32x16_bf16 v[208:223], v[44:47], v[92:95], v[208:223]\n"
        "ds_read_b64_tr_b16 v[40:41], v13 offset:8192\n"
        "ds_read_b64_tr_b16 v[42:43], v13 offset:8704\n"
        "s_setprio 0\n"
        "v_exp_f32 v226, v226\n"
        "v_mul_f32 v230, v230, v198\n"
        "v_exp_f32 v227, v227\n"
        "v_mul_f32 v231, v231, v199\n"
        "s_waitcnt lgkmcnt(4)\n"
        "v_mfma_f32_32x32x16_bf16 v[208:223], v[28:31], v[96:99], v[208:223]\n"
        "ds_read_b64_tr_b16 v[44:45], v10 offset:16384\n"
        "ds_read_b64_tr_b16 v[46:47], v10 offset:16896\n"
        "v_fma_f32 v224, v224, v20, v20\n"
        "v_exp_f32 v228, v228\n"
        "v_fma_f32 v225, v225, v20, v20\n"
        "v_exp_f32 v229, v229\n"
        "v_mfma_f32_16x16x32_bf16 v[236:239], v[232:235], v[28:31], v[236:239]\n"
        "s_add_u32 m0, s91, 0x1c000\n"
        "s_add_u32 s92, s70, 0x0\n"
        "buffer_load_dwordx4 v3, s[44:47], s92 offen lds\n"
        "v_mfma_f32_32x32x16_bf16 v[208:223], v[32:35], v[100:103], v[208:223]\n"
        "ds_read_b64_tr_b16 v[28:29], v11 offset:16384\n"
        "ds_read_b64_tr_b16 v[30:31], v11 offset:16896\n"
        "v_fma_f32 v226, v226, v20, v20\n"
        "v_exp_f32 v230, v230\n"
        "v_fma_f32 v227, v227, v20, v20\n"
        "s_waitcnt lgkmcnt(4)\n"
        "v_mfma_f32_32x32x16_bf16 v[208:223], v[36:39], v[104:107], v[208:223]\n"
        "ds_read_b64_tr_b16 v[32:33], v12 offset:16384\n"
        "ds_read_b64_tr_b16 v[34:35], v12 offset:16896\n"
        "v_exp_f32 v231, v231\n"
        "v_rcp_f32 v224, v224\n"
        "v_fma_f32 v228, v228, v20, v20\n"
        "v_mfma_f32_32x32x16_bf16 v[208:223], v[40:43], v[108:111], v[208:223]\n"
        "ds_read_b64_tr_b16 v[36:37], v13 offset:16384\n"
        "ds_read_b64_tr_b16 v[38:39], v13 offset:16896\n"
        "v_rcp_f32 v225, v225\n"
        "v_fma_f32 v229, v229, v20, v20\n"
        "s_barrier\n"
        "s_setprio 3\n"
        "v_rcp_f32 v226, v226\n"
        "s_add_u32 m0, s91, 0x6000\n"
        "s_add_u32 s92, s70, 0x0\n"
        "buffer_load_dwordx4 v4, s[44:47], s92 offen lds\n"
        "s_waitcnt lgkmcnt(4)\n"
        "v_mfma_f32_32x32x16_bf16 v[208:223], v[44:47], v[112:115], v[208:223]\n"
        "ds_read_b64_tr_b16 v[40:41], v10 offset:24576\n"
        "ds_read_b64_tr_b16 v[42:43], v10 offset:25088\n"
        "v_fma_f32 v230, v230, v20, v20\n"
        "v_rcp_f32 v227, v227\n"
        "v_mfma_f32_16x16x32_bf16 v[236:239], v[232:235], v[44:47], v[236:239]\n"
        "v_mfma_f32_32x32x16_bf16 v[208:223], v[28:31], v[116:119], v[208:223]\n"
        "ds_read_b64_tr_b16 v[44:45], v11 offset:24576\n"
        "ds_read_b64_tr_b16 v[46:47], v11 offset:25088\n"
        "v_fma_f32 v231, v231, v20, v20\n"
        "v_mul_f32 v224, v224, v192\n"
        "v_rcp_f32 v228, v228\n"
        "v_mul_f32 v225, v225, v193\n"
        "v_rcp_f32 v229, v229\n"
        "v_lshlrev_b32 v26, 25, v0\n"
        "v_and_b32 v26, 0x40000000, v26\n"
        "v_lshl_add_u32 v26, v0, 1, v26\n"
        "s_lshl_b32 s93, s22, 6\n"
        "s_add_u32 s93, s93, s66\n"
        "buffer_load_ushort v26, v26, s[56:59], s93 offen offset:384\n"
        "s_waitcnt lgkmcnt(4)\n"
        "v_mfma_f32_32x32x16_bf16 v[208:223], v[32:35], v[120:123], v[208:223]\n"
        "ds_read_b64_tr_b16 v[28:29], v12 offset:24576\n"
        "ds_read_b64_tr_b16 v[30:31], v12 offset:25088\n"
        "v_mul_f32 v226, v226, v194\n"
        "v_rcp_f32 v230, v230\n"
        "v_mul_f32 v227, v227, v195\n"
        "v_mfma_f32_32x32x16_bf16 v[208:223], v[36:39], v[124:127], v[208:223]\n"
        "ds_read_b64_tr_b16 v[32:33], v13 offset:24576\n"
        "ds_read_b64_tr_b16 v[34:35], v13 offset:25088\n"
        "v_rcp_f32 v231, v231\n"
        "v_mul_f32 v228, v228, v196\n"
        "v_mul_f32 v229, v229, v197\n"
        "v_mul_f32 v230, v230, v198\n"
        "v_mul_f32 v231, v231, v199\n"
        "s_waitcnt lgkmcnt(4)\n"
        "v_mfma_f32_32x32x16_bf16 v[208:223], v[40:43], v[128:131], v[208:223]\n"
        "ds_read_b64_tr_b16 v[36:37], v6 offset:49152\n"
        "ds_read_b64_tr_b16 v[38:39], v6 offset:49664\n"
        "s_setprio 2\n"
        "v_cvt_pk_bf16_f32 v56, v224, v225\n"
        "v_cvt_pk_bf16_f32 v57, v226, v227\n"
        "v_cvt_pk_bf16_f32 v58, v228, v229\n"
        "v_cvt_pk_bf16_f32 v59, v230, v231\n"
        "v_mfma_f32_16x16x32_bf16 v[236:239], v[232:235], v[40:43], v[236:239]\n"
        "s_add_u32 m0, s91, 0x8000\n"
        "s_add_u32 s92, s70, 0x4800\n"
        "buffer_load_dwordx4 v3, s[44:47], s92 offen lds\n"
        "s_add_u32 s93, s78, 0x1000\n"
        "buffer_store_dwordx4 v[56:59], v5, s[48:51], s93 offen offset:0 sc1\n"
        "v_mfma_f32_32x32x16_bf16 v[208:223], v[44:47], v[132:135], v[208:223]\n"
        "ds_read_b64_tr_b16 v[40:41], v7 offset:49152\n"
        "ds_read_b64_tr_b16 v[42:43], v7 offset:49664\n"
        "v_mul_f32 v224, v200, v200\n"
        "v_mul_f32 v225, v201, v201\n"
        "v_mul_f32 v226, v202, v202\n"
        "v_mul_f32 v227, v203, v203\n"
        "v_fma_f32 v224, v224, v17, v16\n"
        "v_mul_f32 v228, v204, v204\n"
        "s_waitcnt lgkmcnt(4)\n"
        "v_mfma_f32_32x32x16_bf16 v[208:223], v[28:31], v[136:139], v[208:223]\n"
        "ds_read_b64_tr_b16 v[44:45], v8 offset:49152\n"
        "ds_read_b64_tr_b16 v[46:47], v8 offset:49664\n"
        "v_fma_f32 v225, v225, v17, v16\n"
        "v_mul_f32 v229, v205, v205\n"
        "v_fma_f32 v226, v226, v17, v16\n"
        "v_mul_f32 v230, v206, v206\n"
        "v_fma_f32 v227, v227, v17, v16\n"
        "v_mfma_f32_32x32x16_bf16 v[208:223], v[32:35], v[140:143], v[208:223]\n"
        "ds_read_b64_tr_b16 v[28:29], v9 offset:49152\n"
        "ds_read_b64_tr_b16 v[30:31], v9 offset:49664\n"
        "v_mul_f32 v231, v207, v207\n"
        "v_mul_f32 v224, v224, v200\n"
        "v_fma_f32 v228, v228, v17, v16\n"
        "v_mul_f32 v225, v225, v201\n"
        "v_fma_f32 v229, v229, v17, v16\n"
        "v_mul_f32 v226, v226, v202\n"
        "s_waitcnt lgkmcnt(4)\n"
        "v_mfma_f32_32x32x16_bf16 v[208:223], v[36:39], v[144:147], v[208:223]\n"
        "ds_read_b64_tr_b16 v[32:33], v6 offset:57344\n"
        "ds_read_b64_tr_b16 v[34:35], v6 offset:57856\n"
        "v_fma_f32 v230, v230, v17, v16\n"
        "v_mul_f32 v227, v227, v203\n"
        "v_fma_f32 v231, v231, v17, v16\n"
        "v_exp_f32 v224, v224\n"
        "v_mfma_f32_16x16x32_bf16 v[236:239], v[232:235], v[36:39], v[236:239]\n"
        "v_mfma_f32_32x32x16_bf16 v[208:223], v[40:43], v[148:151], v[208:223]\n"
        "ds_read_b64_tr_b16 v[36:37], v7 offset:57344\n"
        "ds_read_b64_tr_b16 v[38:39], v7 offset:57856\n"
        "s_setprio 1\n"
        "v_mul_f32 v228, v228, v204\n"
        "v_exp_f32 v225, v225\n"
        "v_mul_f32 v229, v229, v205\n"
        "v_exp_f32 v226, v226\n"
        "s_add_u32 m0, s91, 0xa000\n"
        "s_add_u32 s92, s70, 0x12000\n"
        "buffer_load_dwordx4 v3, s[44:47], s92 offen lds\n"
        "s_waitcnt lgkmcnt(4)\n"
        "v_mfma_f32_32x32x16_bf16 v[208:223], v[44:47], v[152:155], v[208:223]\n"
        "ds_read_b64_tr_b16 v[40:41], v8 offset:57344\n"
        "ds_read_b64_tr_b16 v[42:43], v8 offset:57856\n"
        "v_mul_f32 v230, v230, v206\n"
        "v_exp_f32 v227, v227\n"
        "v_mul_f32 v231, v231, v207\n"
        "v_fma_f32 v224, v224, v20, v20\n"
        "v_mfma_f32_32x32x16_bf16 v[208:223], v[28:31], v[156:159], v[208:223]\n"
        "ds_read_b64_tr_b16 v[44:45], v9 offset:57344\n"
        "ds_read_b64_tr_b16 v[46:47], v9 offset:57856\n"
        "v_exp_f32 v228, v228\n"
        "v_fma_f32 v225, v225, v20, v20\n"
        "v_exp_f32 v229, v229\n"
        "s_waitcnt lgkmcnt(4)\n"
        "v_mfma_f32_32x32x16_bf16 v[208:223], v[32:35], v[160:163], v[208:223]\n"
        "ds_read_b64_tr_b16 v[28:29], v10 offset:0\n"
        "ds_read_b64_tr_b16 v[30:31], v10 offset:512\n"
        "v_fma_f32 v226, v226, v20, v20\n"
        "v_exp_f32 v230, v230\n"
        "v_fma_f32 v227, v227, v20, v20\n"
        "v_exp_f32 v231, v231\n"
        "v_mfma_f32_16x16x32_bf16 v[236:239], v[232:235], v[32:35], v[236:239]\n"
        "v_mfma_f32_32x32x16_bf16 v[208:223], v[36:39], v[164:167], v[208:223]\n"
        "ds_read_b64_tr_b16 v[32:33], v11 offset:0\n"
        "ds_read_b64_tr_b16 v[34:35], v11 offset:512\n"
        "v_rcp_f32 v224, v224\n"
        "v_fma_f32 v228, v228, v20, v20\n"
        "v_rcp_f32 v225, v225\n"
        "s_waitcnt lgkmcnt(4)\n"
        "v_mfma_f32_32x32x16_bf16 v[208:223], v[40:43], v[168:171], v[208:223]\n"
        "ds_read_b64_tr_b16 v[36:37], v12 offset:0\n"
        "ds_read_b64_tr_b16 v[38:39], v12 offset:512\n"
        "s_setprio 0\n"
        "v_fma_f32 v229, v229, v20, v20\n"
        "v_rcp_f32 v226, v226\n"
        "v_fma_f32 v230, v230, v20, v20\n"
        "s_add_u32 m0, s91, 0x12000\n"
        "s_add_u32 s92, s70, 0x12000\n"
        "buffer_load_dwordx4 v4, s[44:47], s92 offen lds\n"
        "v_mfma_f32_32x32x16_bf16 v[208:223], v[44:47], v[172:175], v[208:223]\n"
        "ds_read_b64_tr_b16 v[40:41], v13 offset:0\n"
        "ds_read_b64_tr_b16 v[42:43], v13 offset:512\n"
        "v_rcp_f32 v227, v227\n"
        "v_fma_f32 v231, v231, v20, v20\n"
        "v_mul_f32 v224, v224, v200\n"
        "v_rcp_f32 v228, v228\n"
        "s_waitcnt lgkmcnt(4)\n"
        "v_mfma_f32_32x32x16_bf16 v[208:223], v[28:31], v[176:179], v[208:223]\n"
        "v_mul_f32 v225, v225, v201\n"
        "v_rcp_f32 v229, v229\n"
        "v_bfe_u32 v10, v0, 2, 2\n"
        "v_and_b32 v11, 3, v0\n"
        "v_lshlrev_b32 v11, 3, v11\n"
        "v_mul_f32 v226, v226, v202\n"
        "v_and_b32 v12, 16, v0\n"
        "v_mfma_f32_16x16x32_bf16 v[236:239], v[232:235], v[28:31], v[236:239]\n"
        "v_mfma_f32_32x32x16_bf16 v[208:223], v[32:35], v[180:183], v[208:223]\n"
        "v_rcp_f32 v230, v230\n"
        "v_lshl_add_u32 v11, v12, 1, v11\n"
        "v_lshrrev_b32 v12, 5, v0\n"
        "s_lshl_b32 s82, s22, 5\n"
        "v_xor_b32 v11, s82, v11\n"
        "v_mul_f32 v227, v227, v203\n"
        "v_lshl_add_u32 v10, v12, 3, v10\n"
        "v_rcp_f32 v231, v231\n"
        "v_lshl_add_u32 v6, v10, 6, v11\n"
        "s_lshl_b32 s82, s26, 10\n"
        "v_add_u32 v6, s82, v6\n"
        "v_mul_f32 v228, v228, v204\n"
        "s_waitcnt lgkmcnt(2)\n"
        "v_mfma_f32_32x32x16_bf16 v[208:223], v[36:39], v[184:187], v[208:223]\n"
        "v_xor_b32 v7, 0x400, v6\n"
        "v_mul_f32 v229, v229, v205\n"
        "v_add_u32 v8, 0x10000, v6\n"
        "v_mul_f32 v230, v230, v206\n"
        "v_add_u32 v9, 0x10000, v7\n"
        "v_and_b32 v232, 0x3f003f00, v232\n"
        "v_mul_f32 v231, v231, v207\n"
        "v_and_b32 v233, 0x3f003f00, v233\n"
        "v_cvt_pk_bf16_f32 v60, v224, v225\n"
        "v_and_b32 v234, 0x3f003f00, v234\n"
        "v_cvt_pk_bf16_f32 v61, v226, v227\n"
        "v_and_b32 v235, 0x3f003f00, v235\n"
        "s_lshl_b32 s82, %2, 12\n"
        "s_waitcnt lgkmcnt(0)\n"
        "v_mfma_f32_32x32x16_bf16 v[208:223], v[40:43], v[188:191], v[208:223]\n"
        "v_cvt_pk_bf16_f32 v62, v228, v229\n"
        "s_add_u32 s82, s82, 0x1e000\n"
        "v_add_u32 v10, s82, v5\n"
        "v_cvt_pk_bf16_f32 v63, v230, v231\n"
        "s_lshl_b32 s83, s22, 15\n"
        "s_add_u32 s93, s78, 0x1000\n"
        "buffer_store_dwordx4 v[60:63], v5, s[48:51], s93 offen offset:1024 sc1\n"
        "s_sub_u32 s82, s82, s83\n"
        "s_add_u32 s82, s82, 0x4000\n"
        "v_add_u32 v11, s82, v5\n"
        "ds_write_b32 v24, v236 offset:0\n"
        "s_waitcnt lgkmcnt(0)\n"
        "s_waitcnt vmcnt(11)\n"
        "ds_read_b64_tr_b16 v[44:45], v8 offset:32768\n"
        "ds_read_b64_tr_b16 v[46:47], v8 offset:33024\n"
        "ds_read_b64_tr_b16 v[28:29], v9 offset:32768\n"
        "ds_read_b64_tr_b16 v[30:31], v9 offset:33024\n"
        "ds_read_b64_tr_b16 v[32:33], v8 offset:36864\n"
        "ds_read_b64_tr_b16 v[34:35], v8 offset:37120\n"
        "ds_read_b64_tr_b16 v[36:37], v9 offset:36864\n"
        "ds_read_b64_tr_b16 v[38:39], v9 offset:37120\n"
        "s_barrier\n"
        "ds_read2_b32 v[224:225], v25 offset0:0 offset1:32\n"
        "s_setprio 3\n"
        "s_add_u32 m0, s91, 0xc000\n"
        "s_add_u32 s92, s70, 0x16800\n"
        "buffer_load_dwordx4 v3, s[44:47], s92 offen lds\n"
        "s_waitcnt lgkmcnt(5)\n"
        "v_mfma_f32_32x32x16_bf16 v[192:207], v[44:47], v[48:51], 0\n"
        "ds_read_b64_tr_b16 v[40:41], v6 offset:0\n"
        "ds_read_b64_tr_b16 v[42:43], v6 offset:256\n"
        "v_mfma_f32_16x16x32_bf16 v[236:239], v[232:235], v[44:47], 0\n"
        "v_mfma_f32_32x32x16_bf16 v[192:207], v[28:31], v[52:55], v[192:207]\n"
        "ds_read_b64_tr_b16 v[44:45], v7 offset:0\n"
        "ds_read_b64_tr_b16 v[46:47], v7 offset:256\n"
        "s_setprio 2\n"
        "s_waitcnt lgkmcnt(4)\n"
        "s_waitcnt vmcnt(6)\n"
        "v_add_f32 v224, v224, v225\n"
        "v_lshlrev_b32 v26, 16, v26\n"
        "v_cvt_pk_bf16_f32 v229, v224, v26\n"
        "v_lshlrev_b32 v225, 16, v229\n"
        "v_sub_f32 v225, v224, v225\n"
        "v_cvt_pk_bf16_f32 v228, v224, v225\n"
        "v_cvt_pk_bf16_f32 v230, v26, 0\n"
        "v_mov_b32 v231, 0\n"
        "v_cvt_pk_bf16_f32 v224, v21, v21\n"
        "v_and_b32 v227, 0xffff0000, v224\n"
        "v_sub_f32 v227, v21, v227\n"
        "s_add_u32 m0, s91, 0xe000\n"
        "s_add_u32 s92, s70, 0x24000\n"
        "buffer_load_dwordx4 v3, s[44:47], s92 offen lds\n"
        "v_mfma_f32_32x32x16_bf16 v[192:207], v[32:35], v[64:67], v[192:207]\n"
        "ds_read_b64_tr_b16 v[28:29], v6 offset:4096\n"
        "ds_read_b64_tr_b16 v[30:31], v6 offset:4352\n"
        "v_cvt_pk_bf16_f32 v225, v227, v20\n"
        "v_and_b32 v227, 0xffff0000, v225\n"
        "v_sub_f32 v227, v20, v227\n"
        "v_cvt_pk_bf16_f32 v226, v227, 0\n"
        "v_mov_b32 v227, 0\n"
        "s_nop 1\n"
        "v_mfma_f32_32x32x16_bf16 v[208:223], v[228:231], v[224:227], v[208:223]\n"
        "v_mfma_f32_16x16x32_bf16 v[236:239], v[232:235], v[32:35], v[236:239]\n"
        "v_mfma_f32_32x32x16_bf16 v[192:207], v[36:39], v[68:71], v[192:207]\n"
        "ds_read_b64_tr_b16 v[32:33], v7 offset:4096\n"
        "ds_read_b64_tr_b16 v[34:35], v7 offset:4352\n"
        "s_setprio 1\n"
        "s_add_u32 m0, s91, 0x10000\n"
        "s_add_u32 s92, s70, 0x24000\n"
        "buffer_load_dwordx4 v4, s[44:47], s92 offen lds\n"
        "s_waitcnt lgkmcnt(4)\n"
        "v_mfma_f32_32x32x16_bf16 v[192:207], v[40:43], v[80:83], v[192:207]\n"
        "ds_read_b64_tr_b16 v[36:37], v6 offset:8192\n"
        "ds_read_b64_tr_b16 v[38:39], v6 offset:8448\n"
        "v_mul_f32 v224, v208, v208\n"
        "v_mul_f32 v225, v209, v209\n"
        "v_mfma_f32_16x16x32_bf16 v[236:239], v[232:235], v[40:43], v[236:239]\n"
        "s_add_u32 m0, s91, 0x14000\n"
        "s_add_u32 s92, s70, 0x28800\n"
        "buffer_load_dwordx4 v3, s[44:47], s92 offen lds\n"
        "v_mfma_f32_32x32x16_bf16 v[192:207], v[44:47], v[84:87], v[192:207]\n"
        "ds_read_b64_tr_b16 v[40:41], v7 offset:8192\n"
        "ds_read_b64_tr_b16 v[42:43], v7 offset:8448\n"
        "s_setprio 0\n"
        "v_mul_f32 v226, v210, v210\n"
        "v_mul_f32 v227, v211, v211\n"
        "v_fma_f32 v224, v224, v17, v16\n"
        "v_mul_f32 v228, v212, v212\n"
        "v_fma_f32 v225, v225, v17, v16\n"
        "s_waitcnt lgkmcnt(4)\n"
        "v_mfma_f32_32x32x16_bf16 v[192:207], v[28:31], v[96:99], v[192:207]\n"
        "ds_read_b64_tr_b16 v[44:45], v6 offset:12288\n"
        "ds_read_b64_tr_b16 v[46:47], v6 offset:12544\n"
        "v_mul_f32 v229, v213, v213\n"
        "v_fma_f32 v226, v226, v17, v16\n"
        "v_mul_f32 v230, v214, v214\n"
        "v_fma_f32 v227, v227, v17, v16\n"
        "v_mfma_f32_16x16x32_bf16 v[236:239], v[232:235], v[28:31], v[236:239]\n"
        "s_add_u32 m0, s91, 0x16000\n"
        "s_add_u32 s92, s70, 0x36000\n"
        "buffer_load_dwordx4 v3, s[44:47], s92 offen lds\n"
        "v_mfma_f32_32x32x16_bf16 v[192:207], v[32:35], v[100:103], v[192:207]\n"
        "ds_read_b64_tr_b16 v[28:29], v7 offset:12288\n"
        "ds_read_b64_tr_b16 v[30:31], v7 offset:12544\n"
        "v_mul_f32 v231, v215, v215\n"
        "v_mul_f32 v224, v224, v208\n"
        "v_fma_f32 v228, v228, v17, v16\n"
        "v_mul_f32 v225, v225, v209\n"
        "s_barrier\n"
        "s_setprio 3\n"
        "v_fma_f32 v229, v229, v17, v16\n"
        "s_add_u32 m0, s91, 0x0\n"
        "s_add_u32 s92, s70, 0x36000\n"
        "buffer_load_dwordx4 v4, s[44:47], s92 offen lds\n"
        "s_waitcnt lgkmcnt(4)\n"
        "v_mfma_f32_32x32x16_bf16 v[192:207], v[36:39], v[112:115], v[192:207]\n"
        "ds_read_b64_tr_b16 v[32:33], v6 offset:16384\n"
        "ds_read_b64_tr_b16 v[34:35], v6 offset:16640\n"
        "v_mul_f32 v226, v226, v210\n"
        "v_fma_f32 v230, v230, v17, v16\n"
        "v_mul_f32 v227, v227, v211\n"
        "v_fma_f32 v231, v231, v17, v16\n"
        "v_lshlrev_b32 v26, 25, v0\n"
        "v_and_b32 v26, 0x40000000, v26\n"
        "v_lshl_add_u32 v26, v0, 1, v26\n"
        "s_lshl_b32 s93, s22, 5\n"
        "v_xor_b32 v26, s93, v26\n"
        "buffer_load_ushort v26, v26, s[56:59], s67 offen offset:0\n"
        "v_mfma_f32_16x16x32_bf16 v[236:239], v[232:235], v[36:39], v[236:239]\n"
        "v_mfma_f32_32x32x16_bf16 v[192:207], v[40:43], v[116:119], v[192:207]\n"
        "ds_read_b64_tr_b16 v[36:37], v7 offset:16384\n"
        "ds_read_b64_tr_b16 v[38:39], v7 offset:16640\n"
        "s_setprio 2\n"
        "v_exp_f32 v224, v224\n"
        "v_mul_f32 v228, v228, v212\n"
        "v_exp_f32 v225, v225\n"
        "s_waitcnt lgkmcnt(4)\n"
        "v_mfma_f32_32x32x16_bf16 v[192:207], v[44:47], v[128:131], v[192:207]\n"
        "ds_read_b64_tr_b16 v[40:41], v6 offset:20480\n"
        "ds_read_b64_tr_b16 v[42:43], v6 offset:20736\n"
        "v_mul_f32 v229, v229, v213\n"
        "v_exp_f32 v226, v226\n"
        "buffer_load_dwordx2 v[14:15], v23, s[48:51], 0 offen offset:0 sc1\n"
        "v_mfma_f32_16x16x32_bf16 v[236:239], v[232:235], v[44:47], v[236:239]\n"
        "v_mfma_f32_32x32x16_bf16 v[192:207], v[28:31], v[132:135], v[192:207]\n"
        "ds_read_b64_tr_b16 v[44:45], v7 offset:20480\n"
        "ds_read_b64_tr_b16 v[46:47], v7 offset:20736\n"
        "v_mul_f32 v230, v230, v214\n"
        "v_exp_f32 v227, v227\n"
        "v_mul_f32 v231, v231, v215\n"
        "v_fma_f32 v224, v224, v20, v20\n"
        "s_waitcnt lgkmcnt(4)\n"
        "v_mfma_f32_32x32x16_bf16 v[192:207], v[32:35], v[144:147], v[192:207]\n"
        "ds_read_b64_tr_b16 v[28:29], v8 offset:40960\n"
        "ds_read_b64_tr_b16 v[30:31], v8 offset:41216\n"
        "s_setprio 1\n"
        "v_exp_f32 v228, v228\n"
        "v_fma_f32 v225, v225, v20, v20\n"
        "s_waitcnt vmcnt(8)\n"
        "v_mov_b32 v18, 3\n"
        "v_mov_b32 v19, s65\n"
        "buffer_store_dwordx2 v[18:19], v22, s[48:51], 0 offen offset:0 sc1\n"
        "v_exp_f32 v229, v229\n"
        "v_mfma_f32_16x16x32_bf16 v[236:239], v[232:235], v[32:35], v[236:239]\n"
        "s_add_u32 m0, s91, 0x18000\n"
        "s_add_u32 s92, s70, 0x3a800\n"
        "buffer_load_dwordx4 v3, s[44:47], s92 offen lds\n"
        "v_mfma_f32_32x32x16_bf16 v[192:207], v[36:39], v[148:151], v[192:207]\n"
        "ds_read_b64_tr_b16 v[32:33], v9 offset:40960\n"
        "ds_read_b64_tr_b16 v[34:35], v9 offset:41216\n"
        "v_fma_f32 v226, v226, v20, v20\n"
        "v_exp_f32 v230, v230\n"
        "s_waitcnt lgkmcnt(4)\n"
        "v_mfma_f32_32x32x16_bf16 v[192:207], v[40:43], v[160:163], v[192:207]\n"
        "s_setprio 0\n"
        "v_fma_f32 v227, v227, v20, v20\n"
        "v_bfe_u32 v86, v0, 2, 2\n"
        "v_exp_f32 v231, v231\n"
        "v_and_b32 v87, 3, v0\n"
        "v_lshlrev_b32 v87, 3, v87\n"
        "v_and_b32 v6, 16, v0\n"
        "v_lshl_add_u32 v87, v6, 1, v87\n"
        "v_mfma_f32_16x16x32_bf16 v[236:239], v[232:235], v[40:43], v[236:239]\n"
        "v_mfma_f32_32x32x16_bf16 v[192:207], v[44:47], v[164:167], v[192:207]\n"
        "v_rcp_f32 v224, v224\n"
        "v_lshrrev_b32 v6, 5, v0\n"
        "s_lshl_b32 s82, s22, 12\n"
        "v_add_u32 v87, s82, v87\n"
        "v_lshl_add_u32 v6, v6, 2, v86\n"
        "v_fma_f32 v228, v228, v20, v20\n"
        "v_lshrrev_b32 v84, 1, v86\n"
        "v_xor_b32 v84, s22, v84\n"
        "v_rcp_f32 v225, v225\n"
        "v_lshl_add_u32 v84, v84, 6, v87\n"
        "v_lshl_add_u32 v84, v6, 7, v84\n"
        "s_waitcnt lgkmcnt(2)\n"
        "v_mfma_f32_32x32x16_bf16 v[192:207], v[28:31], v[176:179], v[192:207]\n"
        "v_xor_b32 v85, 64, v84\n"
        "s_lshl_b32 s82, s22, 5\n"
        "v_fma_f32 v229, v229, v20, v20\n"
        "v_xor_b32 v87, s82, v87\n"
        "v_xor_b32 v80, s61, v86\n"
        "v_rcp_f32 v226, v226\n"
        "v_lshl_add_u32 v80, v80, 6, v87\n"
        "v_lshl_add_u32 v80, v6, 8, v80\n"
        "v_xor_b32 v81, 64, v80\n"
        "v_xor_b32 v82, 128, v80\n"
        "v_mfma_f32_16x16x32_bf16 v[236:239], v[232:235], v[28:31], v[236:239]\n"
        "s_waitcnt lgkmcnt(0)\n"
        "v_mfma_f32_32x32x16_bf16 v[192:207], v[32:35], v[180:183], v[192:207]\n"
        "v_xor_b32 v83, 192, v80\n"
        "v_fma_f32 v230, v230, v20, v20\n"
        "v_add_u32 v86, 0x10000, v80\n"
        "v_add_u32 v87, 0x10000, v81\n"
        "v_rcp_f32 v227, v227\n"
        "v_add_u32 v6, 0x10000, v82\n"
        "v_add_u32 v7, 0x10000, v83\n"
        "v_add_u32 v8, 0x10000, v84\n"
        "v_add_u32 v9, 0x10000, v85\n"
        "ds_read_b64_tr_b16 v[36:37], v86 offset:49152\n"
        "ds_read_b64_tr_b16 v[38:39], v86 offset:51200\n"
        "ds_read_b64_tr_b16 v[40:41], v87 offset:49152\n"
        "ds_read_b64_tr_b16 v[42:43], v87 offset:51200\n"
        "ds_read_b64_tr_b16 v[44:45], v6 offset:49152\n"
        "ds_read_b64_tr_b16 v[46:47], v6 offset:51200\n"
        "ds_read_b64_tr_b16 v[28:29], v7 offset:49152\n"
        "ds_read_b64_tr_b16 v[30:31], v7 offset:51200\n"
        "s_barrier\n"
        "s_waitcnt vmcnt(2)\n"
        "v_subrev_u32 v14, 2, v14\n"
        "v_cmp_gt_u32 vcc, 16, v14\n"
        "v_cmp_eq_u32 s[96:97], s65, v15\n"
        "s_nop 0\n"
        "s_and_b64 s[96:97], s[96:97], vcc\n"
        "s_cmp_eq_u64 s[96:97], -1\n"
        "s_cbranch_scc0 LP1_%=s\n"
        "LP1_%=r:\n"
        "s_setprio 3\n"
        "v_fma_f32 v231, v231, v20, v20\n"
        "s_add_u32 m0, s91, 0x2000\n"
        "s_add_u32 s92, s74, 0x0\n"
        "buffer_load_dwordx4 v4, s[44:47], s92 offen lds\n"
        "s_waitcnt lgkmcnt(4)\n"
        "v_mfma_f32_32x32x16_bf16 v[128:143], v[36:39], v[240:243], 0\n"
        "ds_read_b64_tr_b16 v[32:33], v84 offset:24576\n"
        "ds_read_b64_tr_b16 v[34:35], v84 offset:25600\n"
        "v_mul_f32 v224, v224, v208\n"
        "v_rcp_f32 v228, v228\n"
        "v_mul_f32 v225, v225, v209\n"
        "v_mfma_f32_32x32x16_bf16 v[144:159], v[40:43], v[240:243], 0\n"
        "ds_read_b64_tr_b16 v[36:37], v85 offset:24576\n"
        "ds_read_b64_tr_b16 v[38:39], v85 offset:25600\n"
        "v_rcp_f32 v229, v229\n"
        "ds_write_b128 v10, v[200:203] offset:0\n"
        "v_mul_f32 v226, v226, v210\n"
        "v_rcp_f32 v230, v230\n"
        "s_add_u32 m0, s91, 0x4000\n"
        "s_add_u32 s92, s74, 0x0\n"
        "buffer_load_dwordx4 v3, s[44:47], s92 offen lds\n"
        "s_waitcnt lgkmcnt(5)\n"
        "v_mfma_f32_32x32x16_bf16 v[160:175], v[44:47], v[240:243], 0\n"
        "ds_read_b64_tr_b16 v[40:41], v80 offset:32768\n"
        "ds_read_b64_tr_b16 v[42:43], v80 offset:34816\n"
        "s_setprio 2\n"
        "v_mul_f32 v227, v227, v211\n"
        "v_rcp_f32 v231, v231\n"
        "v_mfma_f32_32x32x16_bf16 v[176:191], v[28:31], v[240:243], 0\n"
        "ds_read_b64_tr_b16 v[44:45], v81 offset:32768\n"
        "ds_read_b64_tr_b16 v[46:47], v81 offset:34816\n"
        "v_mul_f32 v228, v228, v212\n"
        "v_mul_f32 v229, v229, v213\n"
        "v_mul_f32 v230, v230, v214\n"
        "ds_write_b128 v10, v[204:207] offset:1024\n"
        "v_mul_f32 v231, v231, v215\n"
        "v_cvt_pk_bf16_f32 v72, v224, v225\n"
        "s_add_u32 m0, s91, 0x1a000\n"
        "s_add_u32 s92, s71, 0x0\n"
        "buffer_load_dwordx4 v3, s[44:47], s92 offen lds\n"
        "s_waitcnt lgkmcnt(6)\n"
        "v_mfma_f32_32x32x16_bf16 v[96:111], v[32:35], v[240:243], 0\n"
        "ds_read_b64_tr_b16 v[28:29], v82 offset:32768\n"
        "ds_read_b64_tr_b16 v[30:31], v82 offset:34816\n"
        "v_cvt_pk_bf16_f32 v73, v226, v227\n"
        "v_cvt_pk_bf16_f32 v74, v228, v229\n"
        "v_cvt_pk_bf16_f32 v75, v230, v231\n"
        "s_add_u32 s93, s78, 0x1000\n"
        "buffer_store_dwordx4 v[72:75], v5, s[48:51], s93 offen offset:2048 sc1\n"
        "v_mfma_f32_32x32x16_bf16 v[112:127], v[36:39], v[240:243], 0\n"
        "ds_read_b64_tr_b16 v[32:33], v83 offset:32768\n"
        "ds_read_b64_tr_b16 v[34:35], v83 offset:34816\n"
        "s_setprio 1\n"
        "v_mul_f32 v224, v216, v216\n"
        "v_mul_f32 v225, v217, v217\n"
        "v_mul_f32 v226, v218, v218\n"
        "v_mul_f32 v227, v219, v219\n"
        "v_fma_f32 v224, v224, v17, v16\n"
        "buffer_load_dwordx4 v[52:55], v5, s[48:51], s79 offen offset:0 sc1\n"
        "s_waitcnt lgkmcnt(5)\n"
        "v_mfma_f32_32x32x16_bf16 v[128:143], v[40:43], v[244:247], v[128:143]\n"
        "ds_read_b64_tr_b16 v[36:37], v84 offset:26624\n"
        "ds_read_b64_tr_b16 v[38:39], v84 offset:27648\n"
        "v_mul_f32 v228, v220, v220\n"
        "v_fma_f32 v225, v225, v17, v16\n"
        "v_mul_f32 v229, v221, v221\n"
        "v_fma_f32 v226, v226, v17, v16\n"
        "v_mfma_f32_32x32x16_bf16 v[144:159], v[44:47], v[244:247], v[144:159]\n"
        "ds_read_b64_tr_b16 v[40:41], v85 offset:26624\n"
        "ds_read_b64_tr_b16 v[42:43], v85 offset:27648\n"
        "v_mul_f32 v230, v222, v222\n"
        "v_fma_f32 v227, v227, v17, v16\n"
        "v_mul_f32 v231, v223, v223\n"
        "v_mul_f32 v224, v224, v216\n"
        "v_fma_f32 v228, v228, v17, v16\n"
        "buffer_load_dwordx4 v[64:67], v5, s[48:51], s79 offen offset:1024 sc1\n"
        "s_waitcnt lgkmcnt(4)\n"
        "v_mfma_f32_32x32x16_bf16 v[160:175], v[28:31], v[244:247], v[160:175]\n"
        "ds_read_b64_tr_b16 v[44:45], v80 offset:40960\n"
        "ds_read_b64_tr_b16 v[46:47], v80 offset:43008\n"
        "s_setprio 0\n"
        "v_mul_f32 v225, v225, v217\n"
        "v_fma_f32 v229, v229, v17, v16\n"
        "v_mul_f32 v226, v226, v218\n"
        "v_fma_f32 v230, v230, v17, v16\n"
        "v_mfma_f32_32x32x16_bf16 v[176:191], v[32:35], v[244:247], v[176:191]\n"
        "ds_read_b64_tr_b16 v[28:29], v81 offset:40960\n"
        "ds_read_b64_tr_b16 v[30:31], v81 offset:43008\n"
        "v_mul_f32 v227, v227, v219\n"
        "v_fma_f32 v231, v231, v17, v16\n"
        "v_exp_f32 v224, v224\n"
        "buffer_load_dwordx4 v[68:71], v5, s[48:51], s80 offen offset:0 sc1\n"
        "s_waitcnt lgkmcnt(4)\n"
        "v_mfma_f32_32x32x16_bf16 v[96:111], v[36:39], v[244:247], v[96:111]\n"
        "ds_read_b64_tr_b16 v[32:33], v82 offset:40960\n"
        "ds_read_b64_tr_b16 v[34:35], v82 offset:43008\n"
        "v_mul_f32 v228, v228, v220\n"
        "v_exp_f32 v225, v225\n"
        "v_mul_f32 v229, v229, v221\n"
        "v_mfma_f32_32x32x16_bf16 v[112:127], v[40:43], v[244:247], v[112:127]\n"
        "ds_read_b64_tr_b16 v[36:37], v83 offset:40960\n"
        "ds_read_b64_tr_b16 v[38:39], v83 offset:43008\n"
        "v_exp_f32 v226, v226\n"
        "v_mul_f32 v230, v230, v222\n"
        "s_lshl_b32 s92, s22, 6\n"
        "v_xor_b32 v24, s92, v24\n"
        "ds_write_b32 v24, v236 offset:0\n"
        "s_waitcnt lgkmcnt(0)\n"
        "s_barrier\n"
        "s_setprio 3\n"
        "v_exp_f32 v227, v227\n"
        "s_add_u32 m0, s91, 0x6000\n"
        "s_add_u32 s92, s71, 0x4800\n"
        "buffer_load_dwordx4 v3, s[44:47], s92 offen lds\n"
        "v_mfma_f32_32x32x16_bf16 v[128:143], v[44:47], v[248:251], v[128:143]\n"
        "ds_read_b64_tr_b16 v[40:41], v8 offset:8192\n"
        "ds_read_b64_tr_b16 v[42:43], v8 offset:9216\n"
        "v_mul_f32 v231, v231, v223\n"
        "v_fma_f32 v224, v224, v20, v20\n"
        "v_exp_f32 v228, v228\n"
        "v_mfma_f32_32x32x16_bf16 v[144:159], v[28:31], v[248:251], v[144:159]\n"
        "ds_read_b64_tr_b16 v[44:45], v9 offset:8192\n"
        "ds_read_b64_tr_b16 v[46:47], v9 offset:9216\n"
        "v_fma_f32 v225, v225, v20, v20\n"
        "v_exp_f32 v229, v229\n"
        "v_mfma_f32_32x32x16_bf16 v[160:175], v[32:35], v[248:251], v[160:175]\n"
        "ds_read_b64_tr_b16 v[28:29], v80 offset:49152\n"
        "ds_read_b64_tr_b16 v[30:31], v80 offset:51200\n"
        "s_setprio 2\n"
        "v_fma_f32 v226, v226, v20, v20\n"
        "v_exp_f32 v230, v230\n"
        "s_add_u32 m0, s91, 0x8000\n"
        "s_add_u32 s92, s72, 0x0\n"
        "buffer_load_dwordx4 v3, s[44:47], s92 offen lds\n"
        "v_mfma_f32_32x32x16_bf16 v[176:191], v[36:39], v[248:251], v[176:191]\n"
        "ds_read_b64_tr_b16 v[32:33], v81 offset:49152\n"
        "ds_read_b64_tr_b16 v[34:35], v81 offset:51200\n"
        "v_fma_f32 v227, v227, v20, v20\n"
        "v_exp_f32 v231, v231\n"
        "v_rcp_f32 v224, v224\n"
        "s_waitcnt lgkmcnt(4)\n"
        "v_mfma_f32_32x32x16_bf16 v[96:111], v[40:43], v[248:251], v[96:111]\n"
        "ds_read_b64_tr_b16 v[36:37], v82 offset:49152\n"
        "ds_read_b64_tr_b16 v[38:39], v82 offset:51200\n"
        "v_fma_f32 v228, v228, v20, v20\n"
        "v_rcp_f32 v225, v225\n"
        "s_add_u32 m0, s91, 0x1c000\n"
        "s_add_u32 s92, s72, 0x4800\n"
        "buffer_load_dwordx4 v3, s[44:47], s92 offen lds\n"
        "v_mfma_f32_32x32x16_bf16 v[112:127], v[44:47], v[248:251], v[112:127]\n"
        "ds_read_b64_tr_b16 v[40:41], v83 offset:49152\n"
        "ds_read_b64_tr_b16 v[42:43], v83 offset:51200\n"
        "s_setprio 1\n"
        "v_fma_f32 v229, v229, v20, v20\n"
        "v_rcp_f32 v226, v226\n"
        "v_fma_f32 v230, v230, v20, v20\n"
        "s_waitcnt lgkmcnt(4)\n"
        "v_mfma_f32_32x32x16_bf16 v[128:143], v[28:31], v[252:255], v[128:143]\n"
        "ds_read_b64_tr_b16 v[44:45], v8 offset:10240\n"
        "ds_read_b64_tr_b16 v[46:47], v8 offset:11264\n"
        "v_rcp_f32 v227, v227\n"
        "v_fma_f32 v231, v231, v20, v20\n"
        "v_mul_f32 v224, v224, v216\n"
        "buffer_load_dwordx4 v[240:243], v5, s[48:51], s80 offen offset:1024 sc1\n"
        "v_mfma_f32_32x32x16_bf16 v[144:159], v[32:35], v[252:255], v[144:159]\n"
        "ds_read_b64_tr_b16 v[28:29], v9 offset:10240\n"
        "ds_read_b64_tr_b16 v[30:31], v9 offset:11264\n"
        "v_rcp_f32 v228, v228\n"
        "v_mul_f32 v225, v225, v217\n"
        "v_rcp_f32 v229, v229\n"
        "s_waitcnt lgkmcnt(4)\n"
        "v_mfma_f32_32x32x16_bf16 v[160:175], v[36:39], v[252:255], v[160:175]\n"
        "ds_read_b64_tr_b16 v[32:33], v80 offset:57344\n"
        "ds_read_b64_tr_b16 v[34:35], v80 offset:59392\n"
        "s_setprio 0\n"
        "v_mul_f32 v226, v226, v218\n"
        "v_rcp_f32 v230, v230\n"
        "v_mul_f32 v227, v227, v219\n"
        "v_mfma_f32_32x32x16_bf16 v[176:191], v[40:43], v[252:255], v[176:191]\n"
        "ds_read_b64_tr_b16 v[36:37], v81 offset:57344\n"
        "ds_read_b64_tr_b16 v[38:39], v81 offset:59392\n"
        "v_rcp_f32 v231, v231\n"
        "v_mul_f32 v228, v228, v220\n"
        "v_mul_f32 v229, v229, v221\n"
        "buffer_load_dwordx4 v[244:247], v5, s[48:51], s81 offen offset:0 sc1\n"
        "s_waitcnt lgkmcnt(4)\n"
        "v_mfma_f32_32x32x16_bf16 v[96:111], v[44:47], v[252:255], v[96:111]\n"
        "ds_read_b64_tr_b16 v[40:41], v82 offset:57344\n"
        "ds_read_b64_tr_b16 v[42:43], v82 offset:59392\n"
        "v_mul_f32 v230, v230, v222\n"
        "v_mul_f32 v231, v231, v223\n"
        "v_cvt_pk_bf16_f32 v76, v224, v225\n"
        "v_cvt_pk_bf16_f32 v77, v226, v227\n"
        "v_mfma_f32_32x32x16_bf16 v[112:127], v[28:31], v[252:255], v[112:127]\n"
        "ds_read_b64_tr_b16 v[44:45], v83 offset:57344\n"
        "ds_read_b64_tr_b16 v[46:47], v83 offset:59392\n"
        "v_cvt_pk_bf16_f32 v78, v228, v229\n"
        "v_cvt_pk_bf16_f32 v79, v230, v231\n"
        "s_add_u32 s93, s78, 0x1000\n"
        "buffer_store_dwordx4 v[76:79], v5, s[48:51], s93 offen offset:3072 sc1\n"
        "s_barrier\n"
        "s_mul_i32 s92, s22, 0x240\n"
        "v_xor_b32 v25, s92, v25\n"
        "v_and_b32 v224, 32, v0\n"
        "v_lshl_add_u32 v25, v224, 3, v25\n"
        "ds_read2_b32 v[224:225], v25 offset0:0 offset1:32\n"
        "ds_read2_b32 v[226:227], v25 offset0:64 offset1:96\n"
        "ds_read_b128 v[88:91], v11 offset:0\n"
        "ds_read_b128 v[92:95], v11 offset:1024\n"
        "s_setprio 3\n"
        "s_add_u32 m0, s91, 0xa000\n"
        "s_add_u32 s92, s73, 0x0\n"
        "buffer_load_dwordx4 v3, s[44:47], s92 offen lds\n"
        "s_waitcnt lgkmcnt(8)\n"
        "v_mfma_f32_32x32x16_bf16 v[128:143], v[32:35], v[56:59], v[128:143]\n"
        "ds_read_b64_tr_b16 v[28:29], v8 offset:0\n"
        "ds_read_b64_tr_b16 v[30:31], v8 offset:1024\n"
        "v_mfma_f32_32x32x16_bf16 v[144:159], v[36:39], v[56:59], v[144:159]\n"
        "ds_read_b64_tr_b16 v[32:33], v9 offset:0\n"
        "ds_read_b64_tr_b16 v[34:35], v9 offset:1024\n"
        "s_waitcnt lgkmcnt(6)\n"
        "v_add_f32 v224, v224, v225\n"
        "v_add_f32 v226, v226, v227\n"
        "v_add_f32 v224, v224, v226\n"
        "v_lshlrev_b32 v26, 16, v26\n"
        "s_add_u32 m0, s91, 0xc000\n"
        "s_add_u32 s92, s73, 0x4800\n"
        "buffer_load_dwordx4 v3, s[44:47], s92 offen lds\n"
        "v_mfma_f32_32x32x16_bf16 v[160:175], v[40:43], v[56:59], v[160:175]\n"
        "ds_read_b64_tr_b16 v[36:37], v86 offset:16384\n"
        "ds_read_b64_tr_b16 v[38:39], v86 offset:18432\n"
        "s_setprio 2\n"
        "v_cvt_pk_bf16_f32 v229, v224, v26\n"
        "v_lshlrev_b32 v225, 16, v229\n"
        "v_sub_f32 v225, v224, v225\n"
        "v_cvt_pk_bf16_f32 v228, v224, v225\n"
        "v_cvt_pk_bf16_f32 v230, v26, 0\n"
        "v_mov_b32 v231, 0\n"
        "v_cvt_pk_bf16_f32 v224, v21, v21\n"
        "v_and_b32 v227, 0xffff0000, v224\n"
        "s_add_u32 m0, s91, 0x12000\n"
        "s_add_u32 s92, s71, 0x12000\n"
        "buffer_load_dwordx4 v3, s[44:47], s92 offen lds\n"
        "v_mfma_f32_32x32x16_bf16 v[176:191], v[44:47], v[56:59], v[176:191]\n"
        "ds_read_b64_tr_b16 v[40:41], v87 offset:16384\n"
        "ds_read_b64_tr_b16 v[42:43], v87 offset:18432\n"
        "v_sub_f32 v227, v21, v227\n"
        "v_cvt_pk_bf16_f32 v225, v227, v20\n"
        "v_and_b32 v227, 0xffff0000, v225\n"
        "v_sub_f32 v227, v20, v227\n"
        "buffer_load_dwordx2 v[14:15], v23, s[48:51], 0 offen offset:0 sc1\n"
        "v_cvt_pk_bf16_f32 v226, v227, 0\n"
        "v_mov_b32 v227, 0\n"
        "s_nop 1\n"
        "v_mfma_f32_32x32x16_bf16 v[192:207], v[228:231], v[224:227], v[192:207]\n"
        "s_waitcnt lgkmcnt(4)\n"
        "v_mfma_f32_32x32x16_bf16 v[96:111], v[28:31], v[56:59], v[96:111]\n"
        "ds_read_b64_tr_b16 v[44:45], v6 offset:16384\n"
        "ds_read_b64_tr_b16 v[46:47], v6 offset:18432\n"
        "buffer_load_dwordx4 v[208:211], v5, s[48:51], s81 offen offset:1024 sc1\n"
        "v_mfma_f32_32x32x16_bf16 v[112:127], v[32:35], v[56:59], v[112:127]\n"
        "ds_read_b64_tr_b16 v[28:29], v7 offset:16384\n"
        "ds_read_b64_tr_b16 v[30:31], v7 offset:18432\n"
        "s_setprio 1\n"
        "buffer_load_dwordx4 v[212:215], v5, s[48:51], s79 offen offset:2048 sc1\n"
        "s_waitcnt lgkmcnt(4)\n"
        "v_mfma_f32_32x32x16_bf16 v[128:143], v[36:39], v[60:63], v[128:143]\n"
        "ds_read_b64_tr_b16 v[32:33], v8 offset:2048\n"
        "ds_read_b64_tr_b16 v[34:35], v8 offset:3072\n"
        "v_add_f32 v192, v192, v88\n"
        "v_add_f32 v193, v193, v89\n"
        "v_add_f32 v194, v194, v90\n"
        "v_add_f32 v195, v195, v91\n"
        "v_mfma_f32_32x32x16_bf16 v[144:159], v[40:43], v[60:63], v[144:159]\n"
        "ds_read_b64_tr_b16 v[36:37], v9 offset:2048\n"
        "ds_read_b64_tr_b16 v[38:39], v9 offset:3072\n"
        "v_add_f32 v196, v196, v92\n"
        "v_add_f32 v197, v197, v93\n"
        "v_add_f32 v198, v198, v94\n"
        "v_add_f32 v199, v199, v95\n"
        "v_mul_f32 v224, v192, v192\n"
        "v_mul_f32 v225, v193, v193\n"
        "v_mul_f32 v226, v194, v194\n"
        "v_mul_f32 v227, v195, v195\n"
        "buffer_load_dwordx4 v[216:219], v5, s[48:51], s79 offen offset:3072 sc1\n"
        "s_waitcnt lgkmcnt(4)\n"
        "v_mfma_f32_32x32x16_bf16 v[160:175], v[44:47], v[60:63], v[160:175]\n"
        "ds_read_b64_tr_b16 v[40:41], v86 offset:24576\n"
        "ds_read_b64_tr_b16 v[42:43], v86 offset:26624\n"
        "s_setprio 0\n"
        "v_fma_f32 v224, v224, v17, v16\n"
        "v_mul_f32 v228, v196, v196\n"
        "v_fma_f32 v225, v225, v17, v16\n"
        "v_mul_f32 v229, v197, v197\n"
        "v_fma_f32 v226, v226, v17, v16\n"
        "v_mul_f32 v230, v198, v198\n"
        "v_fma_f32 v227, v227, v17, v16\n"
        "buffer_load_dwordx4 v[220:223], v5, s[48:51], s80 offen offset:2048 sc1\n"
        "v_mfma_f32_32x32x16_bf16 v[176:191], v[28:31], v[60:63], v[176:191]\n"
        "ds_read_b64_tr_b16 v[44:45], v87 offset:24576\n"
        "ds_read_b64_tr_b16 v[46:47], v87 offset:26624\n"
        "v_mul_f32 v231, v199, v199\n"
        "v_mul_f32 v224, v224, v192\n"
        "v_fma_f32 v228, v228, v17, v16\n"
        "v_mul_f32 v225, v225, v193\n"
        "v_fma_f32 v229, v229, v17, v16\n"
        "v_mul_f32 v226, v226, v194\n"
        "s_waitcnt lgkmcnt(4)\n"
        "v_mfma_f32_32x32x16_bf16 v[96:111], v[32:35], v[60:63], v[96:111]\n"
        "ds_read_b64_tr_b16 v[28:29], v6 offset:24576\n"
        "ds_read_b64_tr_b16 v[30:31], v6 offset:26624\n"
        "v_fma_f32 v230, v230, v17, v16\n"
        "v_mul_f32 v227, v227, v195\n"
        "v_fma_f32 v231, v231, v17, v16\n"
        "v_exp_f32 v224, v224\n"
        "v_mul_f32 v228, v228, v196\n"
        "v_exp_f32 v225, v225\n"
        "buffer_load_dwordx4 v[248:251], v5, s[48:51], s80 offen offset:3072 sc1\n"
        "v_mfma_f32_32x32x16_bf16 v[112:127], v[36:39], v[60:63], v[112:127]\n"
        "ds_read_b64_tr_b16 v[32:33], v7 offset:24576\n"
        "ds_read_b64_tr_b16 v[34:35], v7 offset:26624\n"
        "v_mul_f32 v229, v229, v197\n"
        "v_exp_f32 v226, v226\n"
        "s_mov_b64 s[56:57], s[12:13]\n"
        "s_and_b32 s57, s57, 0xffff\n"
        "s_mov_b32 s58, 0x480\n"
        "s_mov_b32 s59, 0x20000\n"
        "s_waitcnt vmcnt(20)\n"
        "s_barrier\n"
        "s_waitcnt vmcnt(5)\n"
        "v_subrev_u32 v14, 3, v14\n"
        "v_cmp_gt_u32 vcc, 16, v14\n"
        "v_cmp_eq_u32 s[96:97], s65, v15\n"
        "s_nop 0\n"
        "s_and_b64 s[96:97], s[96:97], vcc\n"
        "s_cmp_eq_u64 s[96:97], -1\n"
        "s_cbranch_scc0 LP2_%=s\n"
        "LP2_%=r:\n"
        "s_setprio 3\n"
        "v_mul_f32 v230, v230, v198\n"
        "s_add_u32 m0, s91, 0xe000\n"
        "s_add_u32 s92, s71, 0x16800\n"
        "buffer_load_dwordx4 v3, s[44:47], s92 offen lds\n"
        "s_waitcnt lgkmcnt(4)\n"
        "v_mfma_f32_32x32x16_bf16 v[128:143], v[40:43], v[72:75], v[128:143]\n"
        "ds_read_b64_tr_b16 v[36:37], v84 offset:0\n"
        "ds_read_b64_tr_b16 v[38:39], v84 offset:1024\n"
        "v_exp_f32 v227, v227\n"
        "v_mul_f32 v231, v231, v199\n"
        "v_fma_f32 v224, v224, v20, v20\n"
        "v_exp_f32 v228, v228\n"
        "v_mfma_f32_32x32x16_bf16 v[144:159], v[44:47], v[72:75], v[144:159]\n"
        "ds_read_b64_tr_b16 v[40:41], v85 offset:0\n"
        "ds_read_b64_tr_b16 v[42:43], v85 offset:1024\n"
        "v_fma_f32 v225, v225, v20, v20\n"
        "v_exp_f32 v229, v229\n"
        "v_fma_f32 v226, v226, v20, v20\n"
        "v_exp_f32 v230, v230\n"
        "s_waitcnt lgkmcnt(4)\n"
        "v_mfma_f32_32x32x16_bf16 v[160:175], v[28:31], v[72:75], v[160:175]\n"
        "ds_read_b64_tr_b16 v[44:45], v86 offset:32768\n"
        "ds_read_b64_tr_b16 v[46:47], v86 offset:34816\n"
        "s_setprio 2\n"
        "v_fma_f32 v227, v227, v20, v20\n"
        "v_exp_f32 v231, v231\n"
        "v_rcp_f32 v224, v224\n"
        "s_add_u32 m0, s91, 0x10000\n"
        "s_add_u32 s92, s72, 0x12000\n"
        "buffer_load_dwordx4 v3, s[44:47], s92 offen lds\n"
        "v_mfma_f32_32x32x16_bf16 v[176:191], v[32:35], v[72:75], v[176:191]\n"
        "ds_read_b64_tr_b16 v[28:29], v87 offset:32768\n"
        "ds_read_b64_tr_b16 v[30:31], v87 offset:34816\n"
        "v_fma_f32 v228, v228, v20, v20\n"
        "v_rcp_f32 v225, v225\n"
        "v_fma_f32 v229, v229, v20, v20\n"
        "v_rcp_f32 v226, v226\n"
        "s_waitcnt lgkmcnt(4)\n"
        "v_mfma_f32_32x32x16_bf16 v[96:111], v[36:39], v[72:75], v[96:111]\n"
        "ds_read_b64_tr_b16 v[32:33], v6 offset:32768\n"
        "ds_read_b64_tr_b16 v[34:35], v6 offset:34816\n"
        "v_fma_f32 v230, v230, v20, v20\n"
        "v_rcp_f32 v227, v227\n"
        "v_fma_f32 v231, v231, v20, v20\n"
        "v_mul_f32 v224, v224, v192\n"
        "s_add_u32 m0, s91, 0x14000\n"
        "s_add_u32 s92, s72, 0x16800\n"
        "buffer_load_dwordx4 v3, s[44:47], s92 offen lds\n"
        "v_mfma_f32_32x32x16_bf16 v[112:127], v[40:43], v[72:75], v[112:127]\n"
        "ds_read_b64_tr_b16 v[36:37], v7 offset:32768\n"
        "ds_read_b64_tr_b16 v[38:39], v7 offset:34816\n"
        "v_rcp_f32 v228, v228\n"
        "v_mul_f32 v225, v225, v193\n"
        "v_rcp_f32 v229, v229\n"
        "v_mul_f32 v226, v226, v194\n"
        "s_waitcnt lgkmcnt(4)\n"
        "v_mfma_f32_32x32x16_bf16 v[128:143], v[44:47], v[76:79], v[128:143]\n"
        "ds_read_b64_tr_b16 v[40:41], v84 offset:2048\n"
        "ds_read_b64_tr_b16 v[42:43], v84 offset:3072\n"
        "s_setprio 1\n"
        "v_rcp_f32 v230, v230\n"
        "v_mul_f32 v227, v227, v195\n"
        "v_rcp_f32 v231, v231\n"
        "v_mul_f32 v228, v228, v196\n"
        "buffer_load_dwordx4 v[56:59], v5, s[48:51], s81 offen offset:2048 sc1\n"
        "v_mfma_f32_32x32x16_bf16 v[144:159], v[28:31], v[76:79], v[144:159]\n"
        "ds_read_b64_tr_b16 v[44:45], v85 offset:2048\n"
        "ds_read_b64_tr_b16 v[46:47], v85 offset:3072\n"
        "v_mul_f32 v229, v229, v197\n"
        "v_mul_f32 v230, v230, v198\n"
        "v_mul_f32 v231, v231, v199\n"
        "v_cvt_pk_bf16_f32 v48, v224, v225\n"
        "v_cvt_pk_bf16_f32 v49, v226, v227\n"
        "v_cvt_pk_bf16_f32 v50, v228, v229\n"
        "s_waitcnt lgkmcnt(4)\n"
        "v_mfma_f32_32x32x16_bf16 v[160:175], v[32:35], v[76:79], v[160:175]\n"
        "ds_read_b64_tr_b16 v[28:29], v84 offset:8192\n"
        "ds_read_b64_tr_b16 v[30:31], v84 offset:9216\n"
        "v_cvt_pk_bf16_f32 v51, v230, v231\n"
        "s_add_u32 s93, s78, 0x2000\n"
        "buffer_store_dwordx4 v[48:51], v5, s[48:51], s93 offen offset:0 sc1\n"
        "v_mfma_f32_32x32x16_bf16 v[176:191], v[36:39], v[76:79], v[176:191]\n"
        "ds_read_b64_tr_b16 v[32:33], v85 offset:8192\n"
        "ds_read_b64_tr_b16 v[34:35], v85 offset:9216\n"
        "s_setprio 0\n"
        "buffer_load_dwordx4 v[60:63], v5, s[48:51], s81 offen offset:3072 sc1\n"
        "s_waitcnt lgkmcnt(4)\n"
        "v_mfma_f32_32x32x16_bf16 v[96:111], v[40:43], v[76:79], v[96:111]\n"
        "ds_read_b64_tr_b16 v[36:37], v80 offset:16384\n"
        "ds_read_b64_tr_b16 v[38:39], v80 offset:18432\n"
        "v_mfma_f32_32x32x16_bf16 v[112:127], v[44:47], v[76:79], v[112:127]\n"
        "ds_read_b64_tr_b16 v[40:41], v81 offset:16384\n"
        "ds_read_b64_tr_b16 v[42:43], v81 offset:18432\n"
        "s_add_u32 s93, s79, 0x1000\n"
        "buffer_load_dwordx4 v[252:255], v5, s[48:51], s93 offen offset:0 sc1\n"
        "s_waitcnt lgkmcnt(4)\n"
        "v_mfma_f32_32x32x16_bf16 v[96:111], v[28:31], v[48:51], v[96:111]\n"
        "ds_read_b64_tr_b16 v[44:45], v82 offset:16384\n"
        "ds_read_b64_tr_b16 v[46:47], v82 offset:18432\n"
        "v_mfma_f32_32x32x16_bf16 v[112:127], v[32:35], v[48:51], v[112:127]\n"
        "ds_read_b64_tr_b16 v[28:29], v83 offset:16384\n"
        "ds_read_b64_tr_b16 v[30:31], v83 offset:18432\n"
        "s_barrier\n"
        "s_setprio 3\n"
        "s_add_u32 m0, s91, 0x0\n"
        "s_add_u32 s92, s73, 0x12000\n"
        "buffer_load_dwordx4 v3, s[44:47], s92 offen lds\n"
        "s_waitcnt lgkmcnt(4)\n"
        "v_mfma_f32_32x32x16_bf16 v[128:143], v[36:39], v[48:51], v[128:143]\n"
        "ds_read_b64_tr_b16 v[32:33], v86 offset:40960\n"
        "ds_read_b64_tr_b16 v[34:35], v86 offset:43008\n"
        "v_mfma_f32_32x32x16_bf16 v[144:159], v[40:43], v[48:51], v[144:159]\n"
        "ds_read_b64_tr_b16 v[36:37], v87 offset:40960\n"
        "ds_read_b64_tr_b16 v[38:39], v87 offset:43008\n"
        "ds_write_b128 v10, v[112:115] offset:0\n"
        "s_add_u32 m0, s91, 0x2000\n"
        "s_add_u32 s92, s73, 0x16800\n"
        "buffer_load_dwordx4 v3, s[44:47], s92 offen lds\n"
        "s_waitcnt lgkmcnt(5)\n"
        "v_mfma_f32_32x32x16_bf16 v[160:175], v[44:47], v[48:51], v[160:175]\n"
        "ds_read_b64_tr_b16 v[40:41], v6 offset:40960\n"
        "ds_read_b64_tr_b16 v[42:43], v6 offset:43008\n"
        "s_setprio 2\n"
        "s_add_u32 m0, s91, 0x16000\n"
        "s_add_u32 s92, s71, 0x24000\n"
        "buffer_load_dwordx4 v3, s[44:47], s92 offen lds\n"
        "v_mfma_f32_32x32x16_bf16 v[176:191], v[28:31], v[48:51], v[176:191]\n"
        "ds_read_b64_tr_b16 v[44:45], v7 offset:40960\n"
        "ds_read_b64_tr_b16 v[46:47], v7 offset:43008\n"
        "s_add_u32 m0, s91, 0x18000\n"
        "s_add_u32 s92, s71, 0x28800\n"
        "buffer_load_dwordx4 v3, s[44:47], s92 offen lds\n"
        "s_waitcnt lgkmcnt(5)\n"
        "v_mfma_f32_32x32x16_bf16 v[128:143], v[32:35], v[52:55], v[128:143]\n"
        "ds_read_b64_tr_b16 v[28:29], v80 offset:24576\n"
        "ds_read_b64_tr_b16 v[30:31], v80 offset:26624\n"
        "v_mfma_f32_32x32x16_bf16 v[144:159], v[36:39], v[52:55], v[144:159]\n"
        "ds_read_b64_tr_b16 v[32:33], v81 offset:24576\n"
        "ds_read_b64_tr_b16 v[34:35], v81 offset:26624\n"
        "s_setprio 1\n"
        "ds_write_b128 v10, v[116:119] offset:1024\n"
        "s_add_u32 s93, s79, 0x1000\n"
        "buffer_load_dwordx4 v[72:75], v5, s[48:51], s93 offen offset:1024 sc1\n"
        "s_waitcnt lgkmcnt(5)\n"
        "v_mfma_f32_32x32x16_bf16 v[160:175], v[40:43], v[52:55], v[160:175]\n"
        "ds_read_b64_tr_b16 v[36:37], v82 offset:24576\n"
        "ds_read_b64_tr_b16 v[38:39], v82 offset:26624\n"
        "s_add_u32 s93, s80, 0x1000\n"
        "buffer_load_dwordx4 v[76:79], v5, s[48:51], s93 offen offset:0 sc1\n"
        "v_mfma_f32_32x32x16_bf16 v[176:191], v[44:47], v[52:55], v[176:191]\n"
        "ds_read_b64_tr_b16 v[40:41], v83 offset:24576\n"
        "ds_read_b64_tr_b16 v[42:43], v83 offset:26624\n"
        "s_add_u32 s93, s80, 0x1000\n"
        "buffer_load_dwordx4 v[88:91], v5, s[48:51], s93 offen offset:1024 sc1\n"
        "s_waitcnt lgkmcnt(5)\n"
        "v_mfma_f32_32x32x16_bf16 v[128:143], v[28:31], v[64:67], v[128:143]\n"
        "ds_read_b64_tr_b16 v[44:45], v80 offset:32768\n"
        "ds_read_b64_tr_b16 v[46:47], v80 offset:34816\n"
        "s_setprio 0\n"
        "v_mfma_f32_32x32x16_bf16 v[144:159], v[32:35], v[64:67], v[144:159]\n"
        "ds_read_b64_tr_b16 v[28:29], v81 offset:32768\n"
        "ds_read_b64_tr_b16 v[30:31], v81 offset:34816\n"
        "s_add_u32 s93, s81, 0x1000\n"
        "buffer_load_dwordx4 v[92:95], v5, s[48:51], s93 offen offset:0 sc1\n"
        "s_waitcnt lgkmcnt(4)\n"
        "v_mfma_f32_32x32x16_bf16 v[160:175], v[36:39], v[64:67], v[160:175]\n"
        "ds_read_b64_tr_b16 v[32:33], v82 offset:32768\n"
        "ds_read_b64_tr_b16 v[34:35], v82 offset:34816\n"
        "s_add_u32 s93, s81, 0x1000\n"
        "buffer_load_dwordx4 v[192:195], v5, s[48:51], s93 offen offset:1024 sc1\n"
        "v_mfma_f32_32x32x16_bf16 v[176:191], v[40:43], v[64:67], v[176:191]\n"
        "ds_read_b64_tr_b16 v[36:37], v83 offset:32768\n"
        "ds_read_b64_tr_b16 v[38:39], v83 offset:34816\n"
        "s_barrier\n"
        "s_setprio 3\n"
        "s_add_u32 m0, s91, 0x4000\n"
        "s_add_u32 s92, s72, 0x24000\n"
        "buffer_load_dwordx4 v3, s[44:47], s92 offen lds\n"
        "s_waitcnt lgkmcnt(4)\n"
        "v_mfma_f32_32x32x16_bf16 v[128:143], v[44:47], v[68:71], v[128:143]\n"
        "ds_read_b64_tr_b16 v[40:41], v86 offset:49152\n"
        "ds_read_b64_tr_b16 v[42:43], v86 offset:51200\n"
        "v_mfma_f32_32x32x16_bf16 v[144:159], v[28:31], v[68:71], v[144:159]\n"
        "ds_read_b64_tr_b16 v[44:45], v87 offset:49152\n"
        "ds_read_b64_tr_b16 v[46:47], v87 offset:51200\n"
        "ds_write_b128 v10, v[120:123] offset:2048\n"
        "s_waitcnt lgkmcnt(5)\n"
        "v_mfma_f32_32x32x16_bf16 v[160:175], v[32:35], v[68:71], v[160:175]\n"
        "ds_read_b64_tr_b16 v[28:29], v6 offset:49152\n"
        "ds_read_b64_tr_b16 v[30:31], v6 offset:51200\n"
        "s_setprio 2\n"
        "v_mfma_f32_32x32x16_bf16 v[176:191], v[36:39], v[68:71], v[176:191]\n"
        "ds_read_b64_tr_b16 v[32:33], v7 offset:49152\n"
        "ds_read_b64_tr_b16 v[34:35], v7 offset:51200\n"
        "s_add_u32 m0, s91, 0x6000\n"
        "s_add_u32 s92, s72, 0x28800\n"
        "buffer_load_dwordx4 v3, s[44:47], s92 offen lds\n"
        "s_waitcnt lgkmcnt(5)\n"
        "v_mfma_f32_32x32x16_bf16 v[128:143], v[40:43], v[240:243], v[128:143]\n"
        "ds_read_b64_tr_b16 v[36:37], v80 offset:40960\n"
        "ds_read_b64_tr_b16 v[38:39], v80 offset:43008\n"
        "v_mfma_f32_32x32x16_bf16 v[144:159], v[44:47], v[240:243], v[144:159]\n"
        "ds_read_b64_tr_b16 v[40:41], v81 offset:40960\n"
        "ds_read_b64_tr_b16 v[42:43], v81 offset:43008\n"
        "s_setprio 1\n"
        "s_waitcnt vmcnt(13)\n"
        "v_mov_b32 v18, 5\n"
        "v_mov_b32 v19, s65\n"
        "buffer_store_dwordx2 v[18:19], v22, s[48:51], 0 offen offset:0 sc1\n"
        "ds_write_b128 v10, v[124:127] offset:3072\n"
        "s_waitcnt lgkmcnt(5)\n"
        "v_mfma_f32_32x32x16_bf16 v[160:175], v[28:31], v[240:243], v[160:175]\n"
        "ds_read_b64_tr_b16 v[44:45], v82 offset:40960\n"
        "ds_read_b64_tr_b16 v[46:47], v82 offset:43008\n"
        "v_mfma_f32_32x32x16_bf16 v[176:191], v[32:35], v[240:243], v[176:191]\n"
        "ds_read_b64_tr_b16 v[28:29], v83 offset:40960\n"
        "ds_read_b64_tr_b16 v[30:31], v83 offset:43008\n"
        "s_add_u32 m0, s91, 0x1a000\n"
        "s_add_u32 s92, s73, 0x24000\n"
        "buffer_load_dwordx4 v3, s[44:47], s92 offen lds\n"
        "s_waitcnt lgkmcnt(5)\n"
        "v_mfma_f32_32x32x16_bf16 v[128:143], v[36:39], v[244:247], v[128:143]\n"
        "ds_read_b64_tr_b16 v[32:33], v80 offset:49152\n"
        "ds_read_b64_tr_b16 v[34:35], v80 offset:51200\n"
        "s_setprio 0\n"
        "v_mfma_f32_32x32x16_bf16 v[144:159], v[40:43], v[244:247], v[144:159]\n"
        "ds_read_b64_tr_b16 v[36:37], v81 offset:49152\n"
        "ds_read_b64_tr_b16 v[38:39], v81 offset:51200\n"
        "s_waitcnt lgkmcnt(4)\n"
        "v_mfma_f32_32x32x16_bf16 v[160:175], v[44:47], v[244:247], v[160:175]\n"
        "ds_read_b64_tr_b16 v[40:41], v82 offset:49152\n"
        "ds_read_b64_tr_b16 v[42:43], v82 offset:51200\n"
        "v_mfma_f32_32x32x16_bf16 v[176:191], v[28:31], v[244:247], v[176:191]\n"
        "ds_read_b64_tr_b16 v[44:45], v83 offset:49152\n"
        "ds_read_b64_tr_b16 v[46:47], v83 offset:51200\n"
        "s_barrier\n"
        "ds_read_b128 v[112:115], v11 offset:0\n"
        "ds_read_b128 v[116:119], v11 offset:1024\n"
        "ds_read_b128 v[120:123], v11 offset:2048\n"
        "ds_read_b128 v[124:127], v11 offset:3072\n"
        "s_setprio 3\n"
        "s_lshl_b32 s27, s62, 8\n"
        "s_add_u32 m0, s91, 0x8000\n"
        "s_add_u32 s92, s73, 0x28800\n"
        "buffer_load_dwordx4 v3, s[44:47], s92 offen lds\n"
        "s_waitcnt lgkmcnt(8)\n"
        "v_mfma_f32_32x32x16_bf16 v[128:143], v[32:35], v[208:211], v[128:143]\n"
        "ds_read_b64_tr_b16 v[28:29], v86 offset:8192\n"
        "ds_read_b64_tr_b16 v[30:31], v86 offset:10240\n"
        "s_lshl_b32 s28, s61, 6\n"
        "s_add_u32 s27, s27, s28\n"
        "v_mfma_f32_32x32x16_bf16 v[144:159], v[36:39], v[208:211], v[144:159]\n"
        "ds_read_b64_tr_b16 v[32:33], v87 offset:8192\n"
        "ds_read_b64_tr_b16 v[34:35], v87 offset:10240\n"
        "s_lshl_b32 s28, s22, 5\n"
        "s_add_u32 s90, s27, s28\n"
        "s_waitcnt lgkmcnt(4)\n"
        "v_lshlrev_b32 v16, 2, v0\n"
        "v_mfma_f32_32x32x16_bf16 v[160:175], v[40:43], v[208:211], v[160:175]\n"
        "ds_read_b64_tr_b16 v[36:37], v6 offset:8192\n"
        "ds_read_b64_tr_b16 v[38:39], v6 offset:10240\n"
        "s_setprio 2\n"
        "v_add_f32 v96, v96, v112\n"
        "v_add_f32 v97, v97, v113\n"
        "v_and_b32 v17, 31, v0\n"
        "v_add_f32 v98, v98, v114\n"
        "v_mul_u32_u24 v17, 0x480, v17\n"
        "v_add_f32 v99, v99, v115\n"
        "v_add_f32 v100, v100, v116\n"
        "v_lshrrev_b32 v20, 5, v0\n"
        "v_mfma_f32_32x32x16_bf16 v[176:191], v[44:47], v[208:211], v[176:191]\n"
        "ds_read_b64_tr_b16 v[40:41], v7 offset:8192\n"
        "ds_read_b64_tr_b16 v[42:43], v7 offset:10240\n"
        "v_add_f32 v101, v101, v117\n"
        "v_add_f32 v102, v102, v118\n"
        "v_lshl_add_u32 v17, v20, 4, v17\n"
        "v_add_f32 v103, v103, v119\n"
        "v_add_f32 v104, v104, v120\n"
        "v_lshlrev_b32 v20, 3, v20\n"
        "v_add_f32 v105, v105, v121\n"
        "s_add_u32 m0, s91, 0xa000\n"
        "s_add_u32 s92, s71, 0x36000\n"
        "buffer_load_dwordx4 v3, s[44:47], s92 offen lds\n"
        "s_waitcnt lgkmcnt(4)\n"
        "v_mfma_f32_32x32x16_bf16 v[128:143], v[28:31], v[212:215], v[128:143]\n"
        "ds_read_b64_tr_b16 v[44:45], v80 offset:57344\n"
        "ds_read_b64_tr_b16 v[46:47], v80 offset:59392\n"
        "v_add_f32 v106, v106, v122\n"
        "v_mov_b32 v21, 0x3f80\n"
        "v_add_f32 v107, v107, v123\n"
        "v_add_f32 v108, v108, v124\n"
        "v_mov_b32 v24, 0x3f800000\n"
        "v_add_f32 v109, v109, v125\n"
        "s_lshl_b32 s27, s63, 14\n"
        "v_add_f32 v110, v110, v126\n"
        "v_mfma_f32_32x32x16_bf16 v[144:159], v[32:35], v[212:215], v[144:159]\n"
        "ds_read_b64_tr_b16 v[28:29], v81 offset:57344\n"
        "ds_read_b64_tr_b16 v[30:31], v81 offset:59392\n"
        "s_setprio 1\n"
        "v_add_f32 v111, v111, v127\n"
        "s_lshr_b32 s28, s62, 1\n"
        "v_cvt_pk_bf16_f32 v112, v96, v97\n"
        "v_cvt_pk_bf16_f32 v113, v98, v99\n"
        "s_lshl_b32 s28, s28, 2\n"
        "v_cvt_pk_bf16_f32 v114, v100, v101\n"
        "v_cvt_pk_bf16_f32 v115, v102, v103\n"
        "s_add_u32 s28, s28, s61\n"
        "s_waitcnt lgkmcnt(4)\n"
        "v_mfma_f32_32x32x16_bf16 v[160:175], v[36:39], v[212:215], v[160:175]\n"
        "ds_read_b64_tr_b16 v[32:33], v82 offset:57344\n"
        "ds_read_b64_tr_b16 v[34:35], v82 offset:59392\n"
        "v_cvt_pk_bf16_f32 v116, v104, v105\n"
        "v_cvt_pk_bf16_f32 v117, v106, v107\n"
        "s_lshl_b32 s28, s28, 7\n"
        "v_cvt_pk_bf16_f32 v118, v108, v109\n"
        "s_add_u32 s27, s27, s28\n"
        "v_cvt_pk_bf16_f32 v119, v110, v111\n"
        "s_lshl_b32 s93, s63, 2\n"
        "s_add_u32 s93, s93, s60\n"
        "s_lshl_b32 s93, s93, 1\n"
        "s_add_u32 s93, s93, s22\n"
        "s_lshl_b32 s93, s93, 8\n"
        "s_add_u32 s93, s93, s64\n"
        "s_lshl_b32 s93, s93, 7\n"
        "s_add_u32 s93, s93, 0x2000000\n"
        "v_lshrrev_b32 v96, 3, v0\n"
        "v_lshlrev_b32 v96, 11, v96\n"
        "v_and_b32 v97, 0x70, v5\n"
        "v_add_u32 v96, v96, v97\n"
        "buffer_store_dwordx4 v[112:115], v96, s[48:51], s93 offen offset:0 sc1\n"
        "s_add_u32 s93, s93, 0x4000\n"
        "buffer_store_dwordx4 v[116:119], v96, s[48:51], s93 offen offset:0 sc1\n"
        "v_mfma_f32_32x32x16_bf16 v[176:191], v[40:43], v[212:215], v[176:191]\n"
        "ds_read_b64_tr_b16 v[36:37], v83 offset:57344\n"
        "ds_read_b64_tr_b16 v[38:39], v83 offset:59392\n"
        "v_and_b32 v25, 15, v0\n"
        "v_lshlrev_b32 v25, 10, v25\n"
        "v_add_u32 v25, s27, v25\n"
        "s_add_u32 m0, s91, 0x1c000\n"
        "s_add_u32 s92, s71, 0x3a800\n"
        "buffer_load_dwordx4 v3, s[44:47], s92 offen lds\n"
        "s_waitcnt lgkmcnt(4)\n"
        "v_mfma_f32_32x32x16_bf16 v[128:143], v[44:47], v[216:219], v[128:143]\n"
        "ds_read_b64_tr_b16 v[40:41], v86 offset:0\n"
        "ds_read_b64_tr_b16 v[42:43], v86 offset:2048\n"
        "s_setprio 0\n"
        "s_lshl_b32 s20, s62, 5\n"
        "s_lshl_b32 s27, s22, 3\n"
        "s_add_u32 s20, s20, s27\n"
        "v_mfma_f32_32x32x16_bf16 v[144:159], v[28:31], v[216:219], v[144:159]\n"
        "ds_read_b64_tr_b16 v[44:45], v87 offset:0\n"
        "ds_read_b64_tr_b16 v[46:47], v87 offset:2048\n"
        "s_add_u32 s20, s20, 1024\n"
        "s_lshl_b32 s21, s63, 4\n"
        "s_waitcnt lgkmcnt(4)\n"
        "v_mfma_f32_32x32x16_bf16 v[160:175], v[32:35], v[216:219], v[160:175]\n"
        "ds_read_b64_tr_b16 v[28:29], v6 offset:0\n"
        "ds_read_b64_tr_b16 v[30:31], v6 offset:2048\n"
        "s_add_u32 s21, s21, s64\n"
        "s_lshl_b32 s21, s21, 3\n"
        "s_add_u32 s21, s21, %2\n"
        "v_mfma_f32_32x32x16_bf16 v[176:191], v[36:39], v[216:219], v[176:191]\n"
        "ds_read_b64_tr_b16 v[32:33], v7 offset:0\n"
        "ds_read_b64_tr_b16 v[34:35], v7 offset:2048\n"
        "s_lshl_b32 s21, s21, 11\n"
        "s_add_u32 s21, s21, 0x2000000\n"
        "s_waitcnt vmcnt(16)\n"
        "s_barrier\n"
        "s_setprio 3\n"
        "s_lshl_b32 s27, s63, 2\n"
        "s_add_u32 m0, s91, 0xc000\n"
        "s_add_u32 s92, s72, 0x36000\n"
        "buffer_load_dwordx4 v3, s[44:47], s92 offen lds\n"
        "s_waitcnt lgkmcnt(4)\n"
        "v_mfma_f32_32x32x16_bf16 v[128:143], v[40:43], v[220:223], v[128:143]\n"
        "ds_read_b64_tr_b16 v[36:37], v86 offset:16384\n"
        "ds_read_b64_tr_b16 v[38:39], v86 offset:18432\n"
        "s_add_u32 s27, s27, s62\n"
        "s_lshl_b32 s27, s27, 2\n"
        "s_xor_b32 s83, s61, 1\n"
        "v_mfma_f32_32x32x16_bf16 v[144:159], v[44:47], v[220:223], v[144:159]\n"
        "ds_read_b64_tr_b16 v[40:41], v87 offset:16384\n"
        "ds_read_b64_tr_b16 v[42:43], v87 offset:18432\n"
        "s_add_u32 s27, s27, s83\n"
        "s_mul_i32 s27, s27, 24\n"
        "s_add_u32 s27, s27, %2\n"
        "s_waitcnt lgkmcnt(4)\n"
        "v_mfma_f32_32x32x16_bf16 v[160:175], v[28:31], v[220:223], v[160:175]\n"
        "ds_read_b64_tr_b16 v[44:45], v6 offset:16384\n"
        "ds_read_b64_tr_b16 v[46:47], v6 offset:18432\n"
        "s_setprio 2\n"
        "s_lshl_b32 s27, s27, 11\n"
        "s_add_u32 s83, s27, 0x1400000\n"
        "s_lshl_b32 s27, s63, 2\n"
        "v_mfma_f32_32x32x16_bf16 v[176:191], v[32:35], v[220:223], v[176:191]\n"
        "ds_read_b64_tr_b16 v[28:29], v7 offset:16384\n"
        "ds_read_b64_tr_b16 v[30:31], v7 offset:18432\n"
        "s_add_u32 s27, s27, s62\n"
        "s_lshl_b32 s27, s27, 2\n"
        "buffer_load_dwordx2 v[14:15], v23, s[48:51], 0 offen offset:0 sc1\n"
        "s_xor_b32 s84, s61, 2\n"
        "s_add_u32 m0, s91, 0xe000\n"
        "s_add_u32 s92, s72, 0x3a800\n"
        "buffer_load_dwordx4 v3, s[44:47], s92 offen lds\n"
        "s_waitcnt lgkmcnt(4)\n"
        "v_mfma_f32_32x32x16_bf16 v[128:143], v[36:39], v[248:251], v[128:143]\n"
        "ds_read_b64_tr_b16 v[32:33], v80 offset:0\n"
        "ds_read_b64_tr_b16 v[34:35], v80 offset:2048\n"
        "s_add_u32 s27, s27, s84\n"
        "s_mul_i32 s27, s27, 24\n"
        "s_add_u32 s27, s27, %2\n"
        "v_mfma_f32_32x32x16_bf16 v[144:159], v[40:43], v[248:251], v[144:159]\n"
        "ds_read_b64_tr_b16 v[36:37], v81 offset:0\n"
        "ds_read_b64_tr_b16 v[38:39], v81 offset:2048\n"
        "s_setprio 1\n"
        "s_lshl_b32 s27, s27, 11\n"
        "s_add_u32 s84, s27, 0x1404000\n"
        "s_lshl_b32 s27, s63, 2\n"
        "s_add_u32 s27, s27, s62\n"
        "s_waitcnt lgkmcnt(4)\n"
        "v_mfma_f32_32x32x16_bf16 v[160:175], v[44:47], v[248:251], v[160:175]\n"
        "ds_read_b64_tr_b16 v[40:41], v82 offset:0\n"
        "ds_read_b64_tr_b16 v[42:43], v82 offset:2048\n"
        "s_lshl_b32 s27, s27, 2\n"
        "s_xor_b32 s85, s61, 3\n"
        "s_add_u32 s27, s27, s85\n"
        "v_mfma_f32_32x32x16_bf16 v[176:191], v[28:31], v[248:251], v[176:191]\n"
        "ds_read_b64_tr_b16 v[44:45], v83 offset:0\n"
        "ds_read_b64_tr_b16 v[46:47], v83 offset:2048\n"
        "s_mul_i32 s27, s27, 24\n"
        "s_add_u32 s27, s27, %2\n"
        "s_lshl_b32 s27, s27, 11\n"
        "s_add_u32 m0, s91, 0x12000\n"
        "s_add_u32 s92, s73, 0x36000\n"
        "buffer_load_dwordx4 v3, s[44:47], s92 offen lds\n"
        "s_waitcnt lgkmcnt(4)\n"
        "v_mfma_f32_32x32x16_bf16 v[128:143], v[32:35], v[56:59], v[128:143]\n"
        "ds_read_b64_tr_b16 v[28:29], v80 offset:8192\n"
        "ds_read_b64_tr_b16 v[30:31], v80 offset:10240\n"
        "s_setprio 0\n"
        "s_add_u32 s85, s27, 0x1408000\n"
        "s_lshl_b32 s27, s63, 2\n"
        "s_add_u32 s27, s27, s62\n"
        "v_mfma_f32_32x32x16_bf16 v[144:159], v[36:39], v[56:59], v[144:159]\n"
        "ds_read_b64_tr_b16 v[32:33], v81 offset:8192\n"
        "ds_read_b64_tr_b16 v[34:35], v81 offset:10240\n"
        "s_lshl_b32 s27, s27, 2\n"
        "s_xor_b32 s86, s61, 0\n"
        "s_add_u32 s27, s27, s86\n"
        "s_waitcnt lgkmcnt(4)\n"
        "v_mfma_f32_32x32x16_bf16 v[160:175], v[40:43], v[56:59], v[160:175]\n"
        "ds_read_b64_tr_b16 v[36:37], v82 offset:8192\n"
        "ds_read_b64_tr_b16 v[38:39], v82 offset:10240\n"
        "s_mul_i32 s27, s27, 24\n"
        "s_add_u32 s27, s27, %2\n"
        "s_lshl_b32 s27, s27, 11\n"
        "v_mfma_f32_32x32x16_bf16 v[176:191], v[44:47], v[56:59], v[176:191]\n"
        "ds_read_b64_tr_b16 v[40:41], v83 offset:8192\n"
        "ds_read_b64_tr_b16 v[42:43], v83 offset:10240\n"
        "s_add_u32 s86, s27, 0x1400000\n"
        "s_add_u32 s87, s86, 16384\n"
        "s_add_u32 s88, s86, 32768\n"
        "s_waitcnt vmcnt(12)\n"
        "s_barrier\n"
        "s_waitcnt vmcnt(2)\n"
        "v_subrev_u32 v14, 5, v14\n"
        "v_cmp_gt_u32 vcc, 16, v14\n"
        "v_cmp_eq_u32 s[96:97], s65, v15\n"
        "s_nop 0\n"
        "s_and_b64 s[96:97], s[96:97], vcc\n"
        "s_cmp_eq_u64 s[96:97], -1\n"
        "s_cbranch_scc0 LP3_%=s\n"
        "LP3_%=r:\n"
        "s_setprio 3\n"
        "s_add_u32 m0, s91, 0x0\n"
        "s_add_u32 s92, s73, 0x3a800\n"
        "buffer_load_dwordx4 v3, s[44:47], s92 offen lds\n"
        "s_waitcnt lgkmcnt(4)\n"
        "v_mfma_f32_32x32x16_bf16 v[128:143], v[28:31], v[60:63], v[128:143]\n"
        "ds_read_b64_tr_b16 v[44:45], v86 offset:24576\n"
        "ds_read_b64_tr_b16 v[46:47], v86 offset:26624\n"
        "v_mfma_f32_32x32x16_bf16 v[144:159], v[32:35], v[60:63], v[144:159]\n"
        "ds_read_b64_tr_b16 v[28:29], v87 offset:24576\n"
        "ds_read_b64_tr_b16 v[30:31], v87 offset:26624\n"
        "s_add_u32 m0, s91, 0x10000\n"
        "s_add_u32 s92, s75, 0x0\n"
        "buffer_load_dwordx4 v3, s[44:47], s92 offen lds\n"
        "s_waitcnt lgkmcnt(4)\n"
        "v_mfma_f32_32x32x16_bf16 v[160:175], v[36:39], v[60:63], v[160:175]\n"
        "ds_read_b64_tr_b16 v[32:33], v6 offset:24576\n"
        "ds_read_b64_tr_b16 v[34:35], v6 offset:26624\n"
        "s_setprio 2\n"
        "s_add_u32 m0, s91, 0x14000\n"
        "s_add_u32 s92, s76, 0x0\n"
        "buffer_load_dwordx4 v3, s[44:47], s92 offen lds\n"
        "v_mfma_f32_32x32x16_bf16 v[176:191], v[40:43], v[60:63], v[176:191]\n"
        "ds_read_b64_tr_b16 v[36:37], v7 offset:24576\n"
        "ds_read_b64_tr_b16 v[38:39], v7 offset:26624\n"
        "s_waitcnt lgkmcnt(4)\n"
        "v_mfma_f32_32x32x16_bf16 v[128:143], v[44:47], v[252:255], v[128:143]\n"
        "ds_read_b64_tr_b16 v[40:41], v86 offset:32768\n"
        "ds_read_b64_tr_b16 v[42:43], v86 offset:34816\n"
        "s_add_u32 s93, s79, 0x1000\n"
        "buffer_load_dwordx4 v[48:51], v5, s[48:51], s93 offen offset:2048 sc1\n"
        "v_mfma_f32_32x32x16_bf16 v[144:159], v[28:31], v[252:255], v[144:159]\n"
        "ds_read_b64_tr_b16 v[44:45], v87 offset:32768\n"
        "ds_read_b64_tr_b16 v[46:47], v87 offset:34816\n"
        "s_setprio 1\n"
        "v_mov_b32 v18, 6\n"
        "v_mov_b32 v19, s65\n"
        "buffer_store_dwordx2 v[18:19], v22, s[48:51], 0 offen offset:0 sc1\n"
        "s_add_u32 s93, s79, 0x1000\n"
        "buffer_load_dwordx4 v[52:55], v5, s[48:51], s93 offen offset:3072 sc1\n"
        "s_waitcnt lgkmcnt(4)\n"
        "v_mfma_f32_32x32x16_bf16 v[160:175], v[32:35], v[252:255], v[160:175]\n"
        "ds_read_b64_tr_b16 v[28:29], v6 offset:32768\n"
        "ds_read_b64_tr_b16 v[30:31], v6 offset:34816\n"
        "v_mfma_f32_32x32x16_bf16 v[176:191], v[36:39], v[252:255], v[176:191]\n"
        "ds_read_b64_tr_b16 v[32:33], v7 offset:32768\n"
        "ds_read_b64_tr_b16 v[34:35], v7 offset:34816\n"
        "s_add_u32 s93, s80, 0x1000\n"
        "buffer_load_dwordx4 v[56:59], v5, s[48:51], s93 offen offset:2048 sc1\n"
        "s_waitcnt lgkmcnt(4)\n"
        "v_mfma_f32_32x32x16_bf16 v[128:143], v[40:43], v[72:75], v[128:143]\n"
        "ds_read_b64_tr_b16 v[36:37], v80 offset:16384\n"
        "ds_read_b64_tr_b16 v[38:39], v80 offset:18432\n"
        "s_setprio 0\n"
        "s_add_u32 s93, s80, 0x1000\n"
        "buffer_load_dwordx4 v[64:67], v5, s[48:51], s93 offen offset:3072 sc1\n"
        "v_mfma_f32_32x32x16_bf16 v[144:159], v[44:47], v[72:75], v[144:159]\n"
        "ds_read_b64_tr_b16 v[40:41], v81 offset:16384\n"
        "ds_read_b64_tr_b16 v[42:43], v81 offset:18432\n"
        "s_waitcnt lgkmcnt(4)\n"
        "v_mfma_f32_32x32x16_bf16 v[160:175], v[28:31], v[72:75], v[160:175]\n"
        "ds_read_b64_tr_b16 v[44:45], v82 offset:16384\n"
        "ds_read_b64_tr_b16 v[46:47], v82 offset:18432\n"
        "s_add_u32 s93, s81, 0x1000\n"
        "buffer_load_dwordx4 v[68:71], v5, s[48:51], s93 offen offset:2048 sc1\n"
        "v_mfma_f32_32x32x16_bf16 v[176:191], v[32:35], v[72:75], v[176:191]\n"
        "ds_read_b64_tr_b16 v[28:29], v83 offset:16384\n"
        "ds_read_b64_tr_b16 v[30:31], v83 offset:18432\n"
        "s_barrier\n"
        "s_setprio 3\n"
        "s_add_u32 m0, s91, 0x2000\n"
        "s_add_u32 s92, s77, 0x0\n"
        "buffer_load_dwordx4 v3, s[44:47], s92 offen lds\n"
        "s_waitcnt lgkmcnt(4)\n"
        "v_mfma_f32_32x32x16_bf16 v[128:143], v[36:39], v[76:79], v[128:143]\n"
        "ds_read_b64_tr_b16 v[32:33], v80 offset:24576\n"
        "ds_read_b64_tr_b16 v[34:35], v80 offset:26624\n"
        "v_mfma_f32_32x32x16_bf16 v[144:159], v[40:43], v[76:79], v[144:159]\n"
        "ds_read_b64_tr_b16 v[36:37], v81 offset:24576\n"
        "ds_read_b64_tr_b16 v[38:39], v81 offset:26624\n"
        "s_waitcnt lgkmcnt(4)\n"
        "v_mfma_f32_32x32x16_bf16 v[160:175], v[44:47], v[76:79], v[160:175]\n"
        "ds_read_b64_tr_b16 v[40:41], v82 offset:24576\n"
        "ds_read_b64_tr_b16 v[42:43], v82 offset:26624\n"
        "s_setprio 2\n"
        "s_add_u32 s93, s81, 0x1000\n"
        "buffer_load_dwordx4 v[60:63], v5, s[48:51], s93 offen offset:3072 sc1\n"
        "v_mfma_f32_32x32x16_bf16 v[176:191], v[28:31], v[76:79], v[176:191]\n"
        "ds_read_b64_tr_b16 v[44:45], v83 offset:24576\n"
        "ds_read_b64_tr_b16 v[46:47], v83 offset:26624\n"
        "s_waitcnt lgkmcnt(4)\n"
        "v_mfma_f32_32x32x16_bf16 v[128:143], v[32:35], v[88:91], v[128:143]\n"
        "ds_read_b64_tr_b16 v[28:29], v86 offset:40960\n"
        "ds_read_b64_tr_b16 v[30:31], v86 offset:43008\n"
        "s_add_u32 s93, s79, 0x2000\n"
        "buffer_load_dwordx4 v[72:75], v5, s[48:51], s93 offen offset:0 sc1\n"
        "v_mfma_f32_32x32x16_bf16 v[144:159], v[36:39], v[88:91], v[144:159]\n"
        "ds_read_b64_tr_b16 v[32:33], v87 offset:40960\n"
        "ds_read_b64_tr_b16 v[34:35], v87 offset:43008\n"
        "s_setprio 1\n"
        "s_waitcnt lgkmcnt(4)\n"
        "v_mfma_f32_32x32x16_bf16 v[160:175], v[40:43], v[88:91], v[160:175]\n"
        "ds_read_b64_tr_b16 v[36:37], v6 offset:40960\n"
        "ds_read_b64_tr_b16 v[38:39], v6 offset:43008\n"
        "s_add_u32 s93, s80, 0x2000\n"
        "buffer_load_dwordx4 v[96:99], v5, s[48:51], s93 offen offset:0 sc1\n"
        "v_mfma_f32_32x32x16_bf16 v[176:191], v[44:47], v[88:91], v[176:191]\n"
        "ds_read_b64_tr_b16 v[40:41], v7 offset:40960\n"
        "ds_read_b64_tr_b16 v[42:43], v7 offset:43008\n"
        "s_waitcnt lgkmcnt(4)\n"
        "v_mfma_f32_32x32x16_bf16 v[128:143], v[28:31], v[92:95], v[128:143]\n"
        "ds_read_b64_tr_b16 v[44:45], v81 offset:32768\n"
        "ds_read_b64_tr_b16 v[46:47], v81 offset:34816\n"
        "s_setprio 0\n"
        "v_mfma_f32_32x32x16_bf16 v[144:159], v[32:35], v[92:95], v[144:159]\n"
        "ds_read_b64_tr_b16 v[28:29], v81 offset:40960\n"
        "ds_read_b64_tr_b16 v[30:31], v81 offset:43008\n"
        "s_add_u32 s93, s81, 0x2000\n"
        "buffer_load_dwordx4 v[100:103], v5, s[48:51], s93 offen offset:0 sc1\n"
        "s_waitcnt lgkmcnt(4)\n"
        "v_mfma_f32_32x32x16_bf16 v[160:175], v[36:39], v[92:95], v[160:175]\n"
        "ds_read_b64_tr_b16 v[32:33], v87 offset:49152\n"
        "ds_read_b64_tr_b16 v[34:35], v87 offset:51200\n"
        "v_mfma_f32_32x32x16_bf16 v[176:191], v[40:43], v[92:95], v[176:191]\n"
        "ds_read_b64_tr_b16 v[36:37], v81 offset:49152\n"
        "ds_read_b64_tr_b16 v[38:39], v81 offset:51200\n"
        "s_waitcnt vmcnt(4)\n"
        "s_barrier\n"
        "s_setprio 3\n"
        "s_waitcnt lgkmcnt(4)\n"
        "v_mfma_f32_32x32x16_bf16 v[144:159], v[44:47], v[192:195], v[144:159]\n"
        "ds_read_b64_tr_b16 v[40:41], v81 offset:57344\n"
        "ds_read_b64_tr_b16 v[42:43], v81 offset:59392\n"
        "v_mfma_f32_32x32x16_bf16 v[144:159], v[28:31], v[48:51], v[144:159]\n"
        "ds_read_b64_tr_b16 v[44:45], v87 offset:8192\n"
        "ds_read_b64_tr_b16 v[46:47], v87 offset:10240\n"
        "s_waitcnt lgkmcnt(4)\n"
        "v_mfma_f32_32x32x16_bf16 v[144:159], v[32:35], v[52:55], v[144:159]\n"
        "ds_read_b64_tr_b16 v[28:29], v81 offset:0\n"
        "ds_read_b64_tr_b16 v[30:31], v81 offset:2048\n"
        "s_setprio 2\n"
        "v_mfma_f32_32x32x16_bf16 v[144:159], v[36:39], v[56:59], v[144:159]\n"
        "ds_read_b64_tr_b16 v[32:33], v87 offset:0\n"
        "ds_read_b64_tr_b16 v[34:35], v87 offset:2048\n"
        "s_waitcnt lgkmcnt(4)\n"
        "v_mfma_f32_32x32x16_bf16 v[144:159], v[40:43], v[64:67], v[144:159]\n"
        "ds_read_b64_tr_b16 v[36:37], v87 offset:16384\n"
        "ds_read_b64_tr_b16 v[38:39], v87 offset:18432\n"
        "v_mfma_f32_32x32x16_bf16 v[144:159], v[44:47], v[68:71], v[144:159]\n"
        "ds_read_b64_tr_b16 v[40:41], v81 offset:8192\n"
        "ds_read_b64_tr_b16 v[42:43], v81 offset:10240\n"
        "s_setprio 1\n"
        "s_waitcnt lgkmcnt(4)\n"
        "s_waitcnt vmcnt(3)\n"
        "v_mfma_f32_32x32x16_bf16 v[144:159], v[28:31], v[60:63], v[144:159]\n"
        "ds_read_b64_tr_b16 v[44:45], v82 offset:32768\n"
        "ds_read_b64_tr_b16 v[46:47], v82 offset:34816\n"
        "s_waitcnt vmcnt(2)\n"
        "v_mfma_f32_32x32x16_bf16 v[144:159], v[32:35], v[72:75], v[144:159]\n"
        "ds_read_b64_tr_b16 v[28:29], v82 offset:40960\n"
        "ds_read_b64_tr_b16 v[30:31], v82 offset:43008\n"
        "buffer_load_dwordx2 v[14:15], v25, s[48:51], 0 offen offset:0 sc1\n"
        "s_waitcnt lgkmcnt(4)\n"
        "s_waitcnt vmcnt(2)\n"
        "v_mfma_f32_32x32x16_bf16 v[144:159], v[36:39], v[96:99], v[144:159]\n"
        "ds_read_b64_tr_b16 v[32:33], v6 offset:49152\n"
        "ds_read_b64_tr_b16 v[34:35], v6 offset:51200\n"
        "s_setprio 0\n"
        "s_waitcnt vmcnt(1)\n"
        "v_mfma_f32_32x32x16_bf16 v[144:159], v[40:43], v[100:103], v[144:159]\n"
        "ds_read_b64_tr_b16 v[36:37], v82 offset:49152\n"
        "ds_read_b64_tr_b16 v[38:39], v82 offset:51200\n"
        "s_waitcnt lgkmcnt(4)\n"
        "v_mfma_f32_32x32x16_bf16 v[160:175], v[44:47], v[192:195], v[160:175]\n"
        "ds_read_b64_tr_b16 v[40:41], v82 offset:57344\n"
        "ds_read_b64_tr_b16 v[42:43], v82 offset:59392\n"
        "v_mfma_f32_32x32x16_bf16 v[160:175], v[28:31], v[48:51], v[160:175]\n"
        "ds_read_b64_tr_b16 v[44:45], v6 offset:8192\n"
        "ds_read_b64_tr_b16 v[46:47], v6 offset:10240\n"
        "s_nop 2\n"
        "ds_write_b128 v10, v[152:155] offset:0\n"
        "s_waitcnt lgkmcnt(5)\n"
        "v_mfma_f32_32x32x16_bf16 v[160:175], v[32:35], v[52:55], v[160:175]\n"
        "ds_read_b64_tr_b16 v[28:29], v82 offset:0\n"
        "ds_read_b64_tr_b16 v[30:31], v82 offset:2048\n"
        "ds_write_b128 v10, v[156:159] offset:1024\n"
        "s_waitcnt lgkmcnt(0)\n"
        "s_barrier\n"
        "s_waitcnt vmcnt(0)\n"
        "v_subrev_u32 v14, 6, v14\n"
        "v_cmp_gt_u32 vcc, 16, v14\n"
        "v_cmp_eq_u32 s[96:97], s65, v15\n"
        "s_nop 0\n"
        "s_and_b64 s[96:97], s[96:97], vcc\n"
        "s_cmp_eq_u64 s[96:97], -1\n"
        "s_cbranch_scc0 LP4_%=s\n"
        "LP4_%=r:\n"
        "ds_read_b128 v[152:155], v11 offset:0\n"
        "ds_read_b128 v[156:159], v11 offset:1024\n"
        "s_setprio 3\n"
        "v_mfma_f32_32x32x16_bf16 v[160:175], v[36:39], v[56:59], v[160:175]\n"
        "ds_read_b64_tr_b16 v[32:33], v6 offset:0\n"
        "ds_read_b64_tr_b16 v[34:35], v6 offset:2048\n"
        "buffer_load_dwordx4 v[88:91], v5, s[48:51], s21 offen offset:0 sc1\n"
        "buffer_load_dwordx4 v[92:95], v5, s[48:51], s21 offen offset:1024 sc1\n"
        "v_lshrrev_b32 v104, 4, v0\n"
        "v_and_b32 v105, 1, v104\n"
        "v_lshrrev_b32 v104, 1, v104\n"
        "v_lshlrev_b32 v104, 4, v104\n"
        "v_lshl_add_u32 v104, v105, 2, v104\n"
        "buffer_load_dword v106, v104, s[56:59], s20 offen offset:0\n"
        "buffer_load_dwordx2 v[76:77], v20, s[56:59], s90 offen offset:0\n"
        "buffer_load_dwordx2 v[78:79], v20, s[56:59], s90 offen offset:16\n"
        "v_mfma_f32_32x32x16_bf16 v[160:175], v[40:43], v[64:67], v[160:175]\n"
        "ds_read_b64_tr_b16 v[36:37], v6 offset:16384\n"
        "ds_read_b64_tr_b16 v[38:39], v6 offset:18432\n"
        "s_setprio 2\n"
        "s_waitcnt lgkmcnt(4)\n"
        "v_mfma_f32_32x32x16_bf16 v[160:175], v[44:47], v[68:71], v[160:175]\n"
        "ds_read_b64_tr_b16 v[40:41], v82 offset:8192\n"
        "ds_read_b64_tr_b16 v[42:43], v82 offset:10240\n"
        "v_add_f32 v144, v144, v152\n"
        "v_add_f32 v145, v145, v153\n"
        "v_add_f32 v146, v146, v154\n"
        "v_add_f32 v147, v147, v155\n"
        "v_add_f32 v148, v148, v156\n"
        "v_mfma_f32_32x32x16_bf16 v[160:175], v[28:31], v[60:63], v[160:175]\n"
        "ds_read_b64_tr_b16 v[44:45], v83 offset:32768\n"
        "ds_read_b64_tr_b16 v[46:47], v83 offset:34816\n"
        "v_add_f32 v149, v149, v157\n"
        "v_add_f32 v150, v150, v158\n"
        "v_add_f32 v151, v151, v159\n"
        "v_cvt_pk_bf16_f32 v152, v144, v145\n"
        "v_cvt_pk_bf16_f32 v153, v146, v147\n"
        "s_waitcnt lgkmcnt(4)\n"
        "v_mfma_f32_32x32x16_bf16 v[160:175], v[32:35], v[72:75], v[160:175]\n"
        "ds_read_b64_tr_b16 v[28:29], v83 offset:40960\n"
        "ds_read_b64_tr_b16 v[30:31], v83 offset:43008\n"
        "s_setprio 1\n"
        "v_cvt_pk_bf16_f32 v154, v148, v149\n"
        "v_cvt_pk_bf16_f32 v155, v150, v151\n"
        "buffer_store_dwordx4 v[152:155], v5, s[48:51], s83 offen offset:0 sc1\n"
        "v_xor_b32 v144, v152, v153\n"
        "v_xor_b32 v145, v154, v155\n"
        "v_xor_b32 v144, v144, v145\n"
        "v_mfma_f32_32x32x16_bf16 v[160:175], v[36:39], v[96:99], v[160:175]\n"
        "ds_read_b64_tr_b16 v[32:33], v7 offset:49152\n"
        "ds_read_b64_tr_b16 v[34:35], v7 offset:51200\n"
        "v_xor_b32 v144, s65, v144\n"
        "buffer_store_dword v144, v16, s[48:51], s83 offen offset:1024 sc1\n"
        "s_waitcnt lgkmcnt(4)\n"
        "v_mfma_f32_32x32x16_bf16 v[160:175], v[40:43], v[100:103], v[160:175]\n"
        "ds_read_b64_tr_b16 v[36:37], v83 offset:49152\n"
        "ds_read_b64_tr_b16 v[38:39], v83 offset:51200\n"
        "s_setprio 0\n"
        "v_mfma_f32_32x32x16_bf16 v[176:191], v[44:47], v[192:195], v[176:191]\n"
        "ds_read_b64_tr_b16 v[40:41], v83 offset:57344\n"
        "ds_read_b64_tr_b16 v[42:43], v83 offset:59392\n"
        "s_waitcnt lgkmcnt(4)\n"
        "v_mfma_f32_32x32x16_bf16 v[176:191], v[28:31], v[48:51], v[176:191]\n"
        "ds_read_b64_tr_b16 v[44:45], v7 offset:8192\n"
        "ds_read_b64_tr_b16 v[46:47], v7 offset:10240\n"
        "s_nop 1\n"
        "ds_write_b128 v10, v[168:171] offset:2048\n"
        "s_lshl_b32 s92, s63, 7\n"
        "s_add_u32 s92, s92, 0x40000\n"
        "s_add_u32 s93, s64, %2\n"
        "s_cmp_eq_u32 s93, 0\n"
        "s_cselect_b32 s92, s92, 0x7ffffff0\n"
        "v_mov_b32 v18, 0x7ffffff0\n"
        "v_writelane_b32 v18, s92, 0\n"
        "v_mov_b32 v19, s65\n"
        "buffer_store_dword v19, v18, s[48:51], 0 offen offset:0 sc1\n"
        "v_mfma_f32_32x32x16_bf16 v[176:191], v[32:35], v[52:55], v[176:191]\n"
        "ds_read_b64_tr_b16 v[28:29], v83 offset:0\n"
        "ds_read_b64_tr_b16 v[30:31], v83 offset:2048\n"
        "ds_write_b128 v10, v[172:175] offset:3072\n"
        "s_waitcnt lgkmcnt(0)\n"
        "s_barrier\n"
        "ds_read_b128 v[168:171], v11 offset:2048\n"
        "ds_read_b128 v[172:175], v11 offset:3072\n"
        "s_setprio 3\n"
        "v_mfma_f32_32x32x16_bf16 v[176:191], v[36:39], v[56:59], v[176:191]\n"
        "ds_read_b64_tr_b16 v[32:33], v7 offset:0\n"
        "ds_read_b64_tr_b16 v[34:35], v7 offset:2048\n"
        "s_waitcnt vmcnt(5)\n"
        "v_mfma_f32_32x32x16_bf16 v[176:191], v[40:43], v[64:67], v[176:191]\n"
        "ds_read_b64_tr_b16 v[36:37], v7 offset:16384\n"
        "ds_read_b64_tr_b16 v[38:39], v7 offset:18432\n"
        "s_setprio 2\n"
        "v_lshlrev_b32 v108, 16, v88\n"
        "v_and_b32 v109, 0xffff0000, v88\n"
        "v_lshlrev_b32 v88, 16, v92\n"
        "v_and_b32 v92, 0xffff0000, v92\n"
        "v_add_f32 v108, v108, v88\n"
        "s_waitcnt lgkmcnt(4)\n"
        "v_mfma_f32_32x32x16_bf16 v[176:191], v[44:47], v[68:71], v[176:191]\n"
        "ds_read_b64_tr_b16 v[40:41], v83 offset:8192\n"
        "ds_read_b64_tr_b16 v[42:43], v83 offset:10240\n"
        "v_add_f32 v109, v109, v92\n"
        "v_add_f32 v160, v160, v168\n"
        "v_lshlrev_b32 v110, 16, v89\n"
        "v_add_f32 v161, v161, v169\n"
        "v_and_b32 v111, 0xffff0000, v89\n"
        "v_add_f32 v162, v162, v170\n"
        "v_lshlrev_b32 v89, 16, v93\n"
        "v_add_f32 v163, v163, v171\n"
        "v_and_b32 v93, 0xffff0000, v93\n"
        "v_add_f32 v164, v164, v172\n"
        "v_add_f32 v110, v110, v89\n"
        "v_mfma_f32_32x32x16_bf16 v[176:191], v[28:31], v[60:63], v[176:191]\n"
        "ds_read_b64_tr_b16 v[44:45], v80 offset:32768\n"
        "ds_read_b64_tr_b16 v[46:47], v80 offset:34816\n"
        "v_add_f32 v165, v165, v173\n"
        "v_add_f32 v111, v111, v93\n"
        "v_add_f32 v166, v166, v174\n"
        "v_lshlrev_b32 v112, 16, v90\n"
        "v_add_f32 v167, v167, v175\n"
        "v_and_b32 v113, 0xffff0000, v90\n"
        "v_cvt_pk_bf16_f32 v168, v160, v161\n"
        "v_lshlrev_b32 v90, 16, v94\n"
        "v_cvt_pk_bf16_f32 v169, v162, v163\n"
        "v_and_b32 v94, 0xffff0000, v94\n"
        "s_waitcnt lgkmcnt(4)\n"
        "v_mfma_f32_32x32x16_bf16 v[176:191], v[32:35], v[72:75], v[176:191]\n"
        "ds_read_b64_tr_b16 v[28:29], v80 offset:40960\n"
        "ds_read_b64_tr_b16 v[30:31], v80 offset:43008\n"
        "s_setprio 1\n"
        "v_cvt_pk_bf16_f32 v170, v164, v165\n"
        "v_add_f32 v112, v112, v90\n"
        "v_cvt_pk_bf16_f32 v171, v166, v167\n"
        "v_add_f32 v113, v113, v94\n"
        "buffer_store_dwordx4 v[168:171], v5, s[48:51], s84 offen offset:0 sc1\n"
        "v_lshlrev_b32 v114, 16, v91\n"
        "v_xor_b32 v160, v168, v169\n"
        "v_and_b32 v115, 0xffff0000, v91\n"
        "v_xor_b32 v161, v170, v171\n"
        "v_lshlrev_b32 v91, 16, v95\n"
        "v_xor_b32 v160, v160, v161\n"
        "v_and_b32 v95, 0xffff0000, v95\n"
        "v_mfma_f32_32x32x16_bf16 v[176:191], v[36:39], v[96:99], v[176:191]\n"
        "ds_read_b64_tr_b16 v[32:33], v86 offset:49152\n"
        "ds_read_b64_tr_b16 v[34:35], v86 offset:51200\n"
        "v_xor_b32 v160, s65, v160\n"
        "v_add_f32 v114, v114, v91\n"
        "buffer_store_dword v160, v16, s[48:51], s84 offen offset:1024 sc1\n"
        "v_add_f32 v115, v115, v95\n"
        "v_permlane32_swap_b32 v108, v112\n"
        "v_permlane32_swap_b32 v109, v113\n"
        "v_permlane32_swap_b32 v110, v114\n"
        "s_waitcnt lgkmcnt(4)\n"
        "v_mfma_f32_32x32x16_bf16 v[176:191], v[40:43], v[100:103], v[176:191]\n"
        "ds_read_b64_tr_b16 v[36:37], v80 offset:49152\n"
        "ds_read_b64_tr_b16 v[38:39], v80 offset:51200\n"
        "s_setprio 0\n"
        "v_permlane32_swap_b32 v111, v115\n"
        "v_add_f32 v108, v108, v112\n"
        "v_add_f32 v109, v109, v113\n"
        "v_add_f32 v110, v110, v114\n"
        "v_add_f32 v111, v111, v115\n"
        "v_and_b32 v105, 7, v0\n"
        "v_mfma_f32_32x32x16_bf16 v[128:143], v[44:47], v[192:195], v[128:143]\n"
        "ds_read_b64_tr_b16 v[40:41], v80 offset:57344\n"
        "ds_read_b64_tr_b16 v[42:43], v80 offset:59392\n"
        "v_mul_u32_u24 v105, 0x480, v105\n"
        "v_permlane16_swap_b32 v108, v110\n"
        "v_permlane16_swap_b32 v109, v111\n"
        "v_add_f32 v108, v108, v110\n"
        "v_add_f32 v109, v109, v111\n"
        "s_waitcnt lgkmcnt(4)\n"
        "v_mfma_f32_32x32x16_bf16 v[128:143], v[28:31], v[48:51], v[128:143]\n"
        "ds_read_b64_tr_b16 v[44:45], v86 offset:8192\n"
        "ds_read_b64_tr_b16 v[46:47], v86 offset:10240\n"
        "ds_write_b128 v10, v[184:187] offset:0\n"
        "v_add_u32 v105, v105, v104\n"
        "v_and_b32 v104, 8, v0\n"
        "v_lshl_add_u32 v105, v104, 27, v105\n"
        "v_add_f32_dpp v110, v108, v108 row_ror:8 row_mask:0xf bank_mask:0xf\n"
        "v_add_f32_dpp v111, v109, v109 row_ror:8 row_mask:0xf bank_mask:0xf\n"
        "v_lshlrev_b32 v108, 16, v106\n"
        "v_mfma_f32_32x32x16_bf16 v[128:143], v[32:35], v[52:55], v[128:143]\n"
        "ds_read_b64_tr_b16 v[28:29], v80 offset:0\n"
        "ds_read_b64_tr_b16 v[30:31], v80 offset:2048\n"
        "ds_write_b128 v10, v[188:191] offset:1024\n"
        "v_and_b32 v109, 0xffff0000, v106\n"
        "v_add_f32 v108, v108, v110\n"
        "v_add_f32 v109, v109, v111\n"
        "v_cvt_pk_bf16_f32 v108, v108, v109\n"
        "s_lshl_b32 s92, s63, 2\n"
        "s_add_u32 s92, s92, s61\n"
        "s_lshl_b32 s92, s92, 2\n"
        "s_add_u32 s92, s92, s60\n"
        "s_mul_i32 s92, s92, 9216\n"
        "s_add_u32 s92, s92, s20\n"
        "buffer_store_dword v108, v105, s[52:55], s92 offen offset:0\n"
        "s_waitcnt lgkmcnt(0)\n"
        "s_barrier\n"
        "ds_read_b128 v[184:187], v11 offset:0\n"
        "ds_read_b128 v[188:191], v11 offset:1024\n"
        "s_setprio 3\n"
        "v_mfma_f32_32x32x16_bf16 v[128:143], v[36:39], v[56:59], v[128:143]\n"
        "ds_read_b64_tr_b16 v[32:33], v86 offset:0\n"
        "ds_read_b64_tr_b16 v[34:35], v86 offset:2048\n"
        "s_setprio 2\n"
        "v_mfma_f32_32x32x16_bf16 v[128:143], v[40:43], v[64:67], v[128:143]\n"
        "ds_read_b64_tr_b16 v[36:37], v86 offset:16384\n"
        "ds_read_b64_tr_b16 v[38:39], v86 offset:18432\n"
        "s_waitcnt lgkmcnt(4)\n"
        "v_add_f32 v176, v176, v184\n"
        "v_add_f32 v177, v177, v185\n"
        "v_add_f32 v178, v178, v186\n"
        "v_add_f32 v179, v179, v187\n"
        "v_mfma_f32_32x32x16_bf16 v[128:143], v[44:47], v[68:71], v[128:143]\n"
        "ds_read_b64_tr_b16 v[40:41], v80 offset:8192\n"
        "ds_read_b64_tr_b16 v[42:43], v80 offset:10240\n"
        "s_setprio 1\n"
        "v_add_f32 v180, v180, v188\n"
        "v_add_f32 v181, v181, v189\n"
        "v_add_f32 v182, v182, v190\n"
        "v_add_f32 v183, v183, v191\n"
        "v_cvt_pk_bf16_f32 v184, v176, v177\n"
        "v_cvt_pk_bf16_f32 v185, v178, v179\n"
        "v_cvt_pk_bf16_f32 v186, v180, v181\n"
        "v_cvt_pk_bf16_f32 v187, v182, v183\n"
        "v_mfma_f32_32x32x16_bf16 v[128:143], v[28:31], v[60:63], v[128:143]\n"
        "buffer_store_dwordx4 v[184:187], v5, s[48:51], s85 offen offset:0 sc1\n"
        "v_xor_b32 v176, v184, v185\n"
        "v_xor_b32 v177, v186, v187\n"
        "v_xor_b32 v176, v176, v177\n"
        "v_xor_b32 v176, s65, v176\n"
        "buffer_store_dword v176, v16, s[48:51], s85 offen offset:1024 sc1\n"
        "s_waitcnt lgkmcnt(2)\n"
        "v_mfma_f32_32x32x16_bf16 v[128:143], v[32:35], v[72:75], v[128:143]\n"
        "s_setprio 0\n"
        "v_mfma_f32_32x32x16_bf16 v[128:143], v[36:39], v[96:99], v[128:143]\n"
        "s_waitcnt lgkmcnt(0)\n"
        "v_mfma_f32_32x32x16_bf16 v[128:143], v[40:43], v[100:103], v[128:143]\n"
        "buffer_load_dwordx4 v[144:147], v5, s[48:51], s86 offen offset:0 sc1\n"
        "buffer_load_dword v156, v16, s[48:51], s86 offen offset:1024 sc1\n"
        "buffer_load_dwordx4 v[148:151], v5, s[48:51], s87 offen offset:0 sc1\n"
        "buffer_load_dword v157, v16, s[48:51], s87 offen offset:1024 sc1\n"
        "buffer_load_dwordx4 v[152:155], v5, s[48:51], s88 offen offset:0 sc1\n"
        "buffer_load_dword v158, v16, s[48:51], s88 offen offset:1024 sc1\n"
        "s_nop 5\n"
        "ds_write_b128 v10, v[140:143] offset:3072\n"
        "ds_write_b128 v10, v[136:139] offset:2048\n"
        "s_waitcnt lgkmcnt(0)\n"
        "s_barrier\n"
        "ds_read_b128 v[136:139], v11 offset:2048\n"
        "ds_read_b128 v[140:143], v11 offset:3072\n"
        "s_waitcnt vmcnt(14)\n"
        "v_dot2c_f32_bf16 v128, v76, v21\n"
        "v_dot2c_f32_bf16 v129, v76, v24\n"
        "v_dot2c_f32_bf16 v130, v77, v21\n"
        "v_dot2c_f32_bf16 v131, v77, v24\n"
        "v_dot2c_f32_bf16 v132, v78, v21\n"
        "v_dot2c_f32_bf16 v133, v78, v24\n"
        "v_dot2c_f32_bf16 v134, v79, v21\n"
        "v_dot2c_f32_bf16 v135, v79, v24\n"
        "s_waitcnt lgkmcnt(0)\n"
        "v_add_f32 v128, v128, v136\n"
        "v_add_f32 v129, v129, v137\n"
        "v_add_f32 v130, v130, v138\n"
        "v_add_f32 v131, v131, v139\n"
        "v_add_f32 v132, v132, v140\n"
        "v_add_f32 v133, v133, v141\n"
        "v_add_f32 v134, v134, v142\n"
        "v_add_f32 v135, v135, v143\n"
        "s_waitcnt vmcnt(0)\n"
        "v_xor_b32 v136, v144, v145\n"
        "v_xor_b32 v139, v146, v147\n"
        "v_xor_b32 v136, v136, v139\n"
        "v_xor_b32 v136, v136, v156\n"
        "v_xor_b32 v136, s65, v136\n"
        "v_xor_b32 v137, v148, v149\n"
        "v_xor_b32 v139, v150, v151\n"
        "v_xor_b32 v137, v137, v139\n"
        "v_xor_b32 v137, v137, v157\n"
        "v_xor_b32 v137, s65, v137\n"
        "v_xor_b32 v138, v152, v153\n"
        "v_xor_b32 v139, v154, v155\n"
        "v_xor_b32 v138, v138, v139\n"
        "v_xor_b32 v138, v138, v158\n"
        "v_xor_b32 v138, s65, v138\n"
        "v_or3_b32 v136, v136, v137, v138\n"
        "v_cmp_eq_u32 vcc, 0, v136\n"
        "s_nop 0\n"
        "s_cmp_eq_u64 vcc, -1\n"
        "s_cbranch_scc0 LP5_%=s\n"
        "LP5_%=r:\n"
        "v_dot2c_f32_bf16 v128, v144, v21\n"
        "v_dot2c_f32_bf16 v129, v144, v24\n"
        "v_dot2c_f32_bf16 v130, v145, v21\n"
        "v_dot2c_f32_bf16 v131, v145, v24\n"
        "v_dot2c_f32_bf16 v132, v146, v21\n"
        "v_dot2c_f32_bf16 v133, v146, v24\n"
        "v_dot2c_f32_bf16 v134, v147, v21\n"
        "v_dot2c_f32_bf16 v135, v147, v24\n"
        "v_dot2c_f32_bf16 v128, v148, v21\n"
        "v_dot2c_f32_bf16 v129, v148, v24\n"
        "v_dot2c_f32_bf16 v130, v149, v21\n"
        "v_dot2c_f32_bf16 v131, v149, v24\n"
        "v_dot2c_f32_bf16 v132, v150, v21\n"
        "v_dot2c_f32_bf16 v133, v150, v24\n"
        "v_dot2c_f32_bf16 v134, v151, v21\n"
        "v_dot2c_f32_bf16 v135, v151, v24\n"
        "v_dot2c_f32_bf16 v128, v152, v21\n"
        "v_dot2c_f32_bf16 v129, v152, v24\n"
        "v_dot2c_f32_bf16 v130, v153, v21\n"
        "v_dot2c_f32_bf16 v131, v153, v24\n"
        "v_dot2c_f32_bf16 v132, v154, v21\n"
        "v_dot2c_f32_bf16 v133, v154, v24\n"
        "v_dot2c_f32_bf16 v134, v155, v21\n"
        "v_dot2c_f32_bf16 v135, v155, v24\n"
        "s_add_u32 s90, s90, s89\n"
        "v_cvt_pk_bf16_f32 v128, v128, v129\n"
        "v_cvt_pk_bf16_f32 v129, v130, v131\n"
        "v_cvt_pk_bf16_f32 v130, v132, v133\n"
        "v_cvt_pk_bf16_f32 v131, v134, v135\n"
        "s_nop 0\n"
        "v_permlane32_swap_b32 v128, v130\n"
        "v_permlane32_swap_b32 v129, v131\n"
        "buffer_store_dwordx4 v[128:131], v17, s[52:55], s90 offen offset:0\n"
        "s_branch LEND_%=\n"
        "LP1_%=s:\n"
        "s_mov_b32 s95, 0\n"
        "LP1_%=l:\n"
        "s_sleep 1\n"
        "buffer_load_dwordx2 v[14:15], v23, s[48:51], 0 offen offset:0 sc1\n"
        "s_waitcnt vmcnt(0)\n"
        "v_subrev_u32 v14, 2, v14\n"
        "v_cmp_gt_u32 vcc, 16, v14\n"
        "v_cmp_eq_u32 s[96:97], s65, v15\n"
        "s_nop 0\n"
        "s_and_b64 s[96:97], s[96:97], vcc\n"
        "s_cmp_eq_u64 s[96:97], -1\n"
        "s_cbranch_scc1 LP1_%=x\n"
        "s_add_u32 s95, s95, 1\n"
        "s_cmp_lt_u32 s95, 0x4000\n"
        "s_cbranch_scc1 LP1_%=l\n"
        "LP1_%=x:\n"
        "s_nop 3\n"
        "s_branch LP1_%=r\n"
        "LP2_%=s:\n"
        "s_mov_b32 s95, 0\n"
        "LP2_%=l:\n"
        "s_sleep 1\n"
        "buffer_load_dwordx2 v[14:15], v23, s[48:51], 0 offen offset:0 sc1\n"
        "s_waitcnt vmcnt(0)\n"
        "v_subrev_u32 v14, 3, v14\n"
        "v_cmp_gt_u32 vcc, 16, v14\n"
        "v_cmp_eq_u32 s[96:97], s65, v15\n"
        "s_nop 0\n"
        "s_and_b64 s[96:97], s[96:97], vcc\n"
        "s_cmp_eq_u64 s[96:97], -1\n"
        "s_cbranch_scc1 LP2_%=x\n"
        "s_add_u32 s95, s95, 1\n"
        "s_cmp_lt_u32 s95, 0x4000\n"
        "s_cbranch_scc1 LP2_%=l\n"
        "LP2_%=x:\n"
        "s_nop 3\n"
        "s_branch LP2_%=r\n"
        "LP3_%=s:\n"
        "s_mov_b32 s95, 0\n"
        "LP3_%=l:\n"
        "s_sleep 1\n"
        "buffer_load_dwordx2 v[14:15], v23, s[48:51], 0 offen offset:0 sc1\n"
        "s_waitcnt vmcnt(0)\n"
        "v_subrev_u32 v14, 5, v14\n"
        "v_cmp_gt_u32 vcc, 16, v14\n"
        "v_cmp_eq_u32 s[96:97], s65, v15\n"
        "s_nop 0\n"
        "s_and_b64 s[96:97], s[96:97], vcc\n"
        "s_cmp_eq_u64 s[96:97], -1\n"
        "s_cbranch_scc1 LP3_%=x\n"
        "s_add_u32 s95, s95, 1\n"
        "s_cmp_lt_u32 s95, 0x4000\n"
        "s_cbranch_scc1 LP3_%=l\n"
        "LP3_%=x:\n"
        "s_nop 3\n"
        "s_branch LP3_%=r\n"
        "LP4_%=s:\n"
        "s_mov_b32 s95, 0\n"
        "LP4_%=l:\n"
        "s_sleep 1\n"
        "buffer_load_dwordx2 v[14:15], v25, s[48:51], 0 offen offset:0 sc1\n"
        "s_waitcnt vmcnt(0)\n"
        "v_subrev_u32 v14, 6, v14\n"
        "v_cmp_gt_u32 vcc, 16, v14\n"
        "v_cmp_eq_u32 s[96:97], s65, v15\n"
        "s_nop 0\n"
        "s_and_b64 s[96:97], s[96:97], vcc\n"
        "s_cmp_eq_u64 s[96:97], -1\n"
        "s_cbranch_scc1 LP4_%=x\n"
        "s_add_u32 s95, s95, 1\n"
        "s_cmp_lt_u32 s95, 0x4000\n"
        "s_cbranch_scc1 LP4_%=l\n"
        "LP4_%=x:\n"
        "s_nop 3\n"
        "s_branch LP4_%=r\n"
        "LP5_%=s:\n"
        "s_mov_b32 s95, 0\n"
        "LP5_%=l:\n"
        "buffer_load_dwordx4 v[144:147], v5, s[48:51], s86 offen offset:0 sc1\n"
        "buffer_load_dword v156, v16, s[48:51], s86 offen offset:1024 sc1\n"
        "buffer_load_dwordx4 v[148:151], v5, s[48:51], s87 offen offset:0 sc1\n"
        "buffer_load_dword v157, v16, s[48:51], s87 offen offset:1024 sc1\n"
        "buffer_load_dwordx4 v[152:155], v5, s[48:51], s88 offen offset:0 sc1\n"
        "buffer_load_dword v158, v16, s[48:51], s88 offen offset:1024 sc1\n"
        "s_waitcnt vmcnt(0)\n"
        "v_xor_b32 v136, v144, v145\n"
        "v_xor_b32 v139, v146, v147\n"
        "v_xor_b32 v136, v136, v139\n"
        "v_xor_b32 v136, v136, v156\n"
        "v_xor_b32 v136, s65, v136\n"
        "v_xor_b32 v137, v148, v149\n"
        "v_xor_b32 v139, v150, v151\n"
        "v_xor_b32 v137, v137, v139\n"
        "v_xor_b32 v137, v137, v157\n"
        "v_xor_b32 v137, s65, v137\n"
        "v_xor_b32 v138, v152, v153\n"
        "v_xor_b32 v139, v154, v155\n"
        "v_xor_b32 v138, v138, v139\n"
        "v_xor_b32 v138, v138, v158\n"
        "v_xor_b32 v138, s65, v138\n"
        "v_or3_b32 v136, v136, v137, v138\n"
        "v_cmp_eq_u32 vcc, 0, v136\n"
        "s_nop 0\n"
        "s_cmp_eq_u64 vcc, -1\n"
        "s_cbranch_scc1 LP5_%=x\n"
        "s_add_u32 s95, s95, 1\n"
        "s_cmp_lt_u32 s95, 0x4000\n"
        "s_cbranch_scc1 LP5_%=l\n"
        "LP5_%=x:\n"
        "s_nop 3\n"
        "s_branch LP5_%=r\n"
        "LEND_%=:\n"
        :
        : "s"(__builtin_amdgcn_kernarg_segment_ptr()), "s"(bid), "s"(wv),
          "s"((unsigned)(size_t)(__attribute__((address_space(3))) char*)smem)
        : "v0", "v1", "v2", "v3", "v4", "v5", "v6", "v7", "v8", "v9", "v10", "v11", "v12", "v13", "v14", "v15", "v16", "v17", "v18", "v19", "v20", "v21", "v22", "v23", "v24", "v25", "v26", "v27", "v28", "v29", "v30", "v31", "v32", "v33", "v34", "v35", "v36", "v37", "v38", "v39", "v40", "v41", "v42", "v43", "v44", "v45", "v46", "v47", "v48", "v49", "v50", "v51", "v52", "v53", "v54", "v55", "v56", "v57", "v58", "v59", "v60", "v61", "v62", "v63", "v64", "v65", "v66", "v67", "v68", "v69", "v70", "v71", "v72", "v73", "v74", "v75", "v76", "v77", "v78", "v79", "v80", "v81", "v82", "v83", "v84", "v85", "v86", "v87", "v88", "v89", "v90", "v91", "v92", "v93", "v94", "v95", "v96", "v97", "v98", "v99", "v100", "v101", "v102", "v103", "v104", "v105", "v106", "v107", "v108", "v109", "v110", "v111", "v112", "v113", "v114", "v115", "v116", "v117", "v118", "v119", "v120", "v121", "v122", "v123", "v124", "v125", "v126", "v127", "v128", "v129", "v130", "v131", "v132", "v133", "v134", "v135", "v136", "v137", "v138", "v139", "v140", "v141", "v142", "v143", "v144", "v145", "v146", "v147", "v148", "v149", "v150", "v151", "v152", "v153", "v154", "v155", "v156", "v157", "v158", "v159", "v160", "v161", "v162", "v163", "v164", "v165", "v166", "v167", "v168", "v169", "v170", "v171", "v172", "v173", "v174", "v175", "v176", "v177", "v178", "v179", "v180", "v181", "v182", "v183", "v184", "v185", "v186", "v187", "v188", "v189", "v190", "v191", "v192", "v193", "v194", "v195", "v196", "v197", "v198", "v199", "v200", "v201", "v202", "v203", "v204", "v205", "v206", "v207", "v208", "v209", "v210", "v211", "v212", "v213", "v214", "v215", "v216", "v217", "v218", "v219", "v220", "v221", "v222", "v223", "v224", "v225", "v226", "v227", "v228", "v229", "v230", "v231", "v232", "v233", "v234", "v235", "v236", "v237", "v238", "v239", "v240", "v241", "v242", "v243", "v244", "v245", "v246", "v247", "v248", "v249", "v250", "v251", "v252", "v253", "v254", "v255", "s4", "s5", "s6", "s7", "s8", "s9", "s10", "s11", "s12", "s13", "s14", "s15", "s16", "s17", "s20", "s21", "s22", "s23", "s24", "s25", "s26", "s27", "s28", "s29", "s30", "s31", "s36", "s37", "s38", "s39", "s40", "s41", "s42", "s43", "s44", "s45", "s46", "s47", "s48", "s49", "s50", "s51", "s52", "s53", "s54", "s55", "s56", "s57", "s58", "s59", "s60", "s61", "s62", "s63", "s64", "s65", "s66", "s67", "s68", "s69", "s70", "s71", "s72", "s73", "s74", "s75", "s76", "s77", "s78", "s79", "s80", "s81", "s82", "s83", "s84", "s85", "s86", "s87", "s88", "s89", "s90", "s91", "s92", "s93", "s94", "s95", "s96", "s97", "s98", "s99", "vcc", "scc", "memory");
}

extern "C" void kernel_launch(void* const* d_in, const int* in_sizes, int n_in,
                              void* d_out, int out_size, void* d_ws, size_t ws_size,
                              hipStream_t stream) {
    mixer_mlp_kernel<<<256, 512, 0, stream>>>(d_in[0], d_in[1], d_in[2], d_in[3], d_in[4], d_out, (char*)d_ws + (0));
}
```
